```python
import torch

def _softmax(x, axis=-1):
    return torch.softmax(x, dim=axis)
def _gelu(x):
    return torch.nn.functional.gelu(x, approximate="tanh")
def _silu(x):
    return torch.nn.functional.silu(x)
def _sigmoid(x):
    return torch.sigmoid(x)
def _layernorm(x, axis=-1, eps=1e-5):
    m = x.mean(dim=axis, keepdim=True)
    v = x.var(dim=axis, keepdim=True, unbiased=False)
    return (x - m) / torch.sqrt(v + eps)
def _scatter_add(tgt, idx, src, axis=0):
    return tgt.clone().index_add_(axis, idx, src)
def _segment_sum(src, seg, n):
    out = torch.zeros((n,) + src.shape[1:], dtype=src.dtype)
    return out.index_add_(0, seg, src)
def _pad(x, pw):
    flat = tuple(v for pair in reversed(pw) for v in pair)
    return torch.nn.functional.pad(x, flat)

kernel_name = 'standalone__glu_gated_glu__prompt'

def setup_inputs():

    _w = (lambda a: (lambda t: t.to(torch.bfloat16) if t.is_floating_point() else t)(torch.as_tensor(a)))
    B, S, D, F = 1, 2048, 448, 1792
    return {
        "x": _w((lambda *s: torch.randn(*s, dtype=torch.bfloat16) * 0.2)(B, S, D)),
        "w_gate": _w((lambda *s: torch.randn(*s, dtype=torch.bfloat16) * 0.2)(D, F)),
        "w_up": _w((lambda *s: torch.randn(*s, dtype=torch.bfloat16) * 0.05)(D, F)),
        "w_down": _w((lambda *s: torch.randn(*s, dtype=torch.bfloat16) * 0.05)(F, D)),
    }

def reference(x, w_gate, w_up, w_down):
    g = torch.einsum("bsd,df->bsf", x, w_gate)
    u = torch.einsum("bsd,df->bsf", x, w_up)
    h = _sigmoid(g) * u
    return torch.einsum("bsf,fd->bsd", h, w_down)
```

```cpp
#include <hip/hip_runtime.h>
#include <hip/hip_bf16.h>
#include <stdint.h>

typedef __attribute__((ext_vector_type(8))) short bf16x8;
typedef __attribute__((ext_vector_type(16))) float f32x16;
typedef __attribute__((ext_vector_type(4))) float f32x4;

static constexpr int S = 2048, D = 448, F = 1792;

__device__ __forceinline__ float bf2f(unsigned short v) { return __uint_as_float(((unsigned)v) << 16); }
__device__ __forceinline__ unsigned short f2bf(float f) { __hip_bfloat16 b = __float2bfloat16(f); return *(unsigned short*)&b; }

__global__ void k1(const unsigned short* x, const unsigned short* wg, const unsigned short* wu, unsigned short* h) {
    int lane = threadIdx.x & 63;
    int r = lane & 31, hh = lane >> 5;
    int tm = blockIdx.x, tn = blockIdx.y;
    f32x16 ag = {}, au = {};
    for (int k0 = 0; k0 < D; k0 += 16) {
        bf16x8 a = *(const bf16x8*)(x + (size_t)(tm * 32 + r) * D + k0 + 8 * hh);
        bf16x8 bg, bu;
        for (int j = 0; j < 8; ++j) {
            bg[j] = wg[(size_t)(k0 + 8 * hh + j) * F + tn * 32 + r];
            bu[j] = wu[(size_t)(k0 + 8 * hh + j) * F + tn * 32 + r];
        }
        ag = __builtin_amdgcn_mfma_f32_32x32x16_bf16(a, bg, ag, 0, 0, 0);
        au = __builtin_amdgcn_mfma_f32_32x32x16_bf16(a, bu, au, 0, 0, 0);
    }
    for (int i = 0; i < 16; ++i) {
        int row = (i & 3) + 8 * (i >> 2) + 4 * hh;
        float g = ag[i], u = au[i];
        float s = 1.0f / (1.0f + __expf(-g));
        h[(size_t)(tm * 32 + row) * F + tn * 32 + r] = f2bf(s * u);
    }
}
__global__ void k2(const unsigned short* h, const unsigned short* wd, unsigned short* out) {
    int lane = threadIdx.x & 63;
    int r = lane & 31, hh = lane >> 5;
    int tm = blockIdx.x, tn = blockIdx.y;
    f32x16 acc = {};
    for (int k0 = 0; k0 < F; k0 += 16) {
        bf16x8 a = *(const bf16x8*)(h + (size_t)(tm * 32 + r) * F + k0 + 8 * hh);
        bf16x8 b;
        for (int j = 0; j < 8; ++j) b[j] = wd[(size_t)(k0 + 8 * hh + j) * D + tn * 32 + r];
        acc = __builtin_amdgcn_mfma_f32_32x32x16_bf16(a, b, acc, 0, 0, 0);
    }
    for (int i = 0; i < 16; ++i) {
        int row = (i & 3) + 8 * (i >> 2) + 4 * hh;
        out[(size_t)(tm * 32 + row) * D + tn * 32 + r] = f2bf(acc[i]);
    }
}

extern "C" void kernel_launch(void* const* d_in, const int* in_sizes, int n_in,
                              void* d_out, int out_size, void* d_ws, size_t ws_size,
                              hipStream_t stream) {
    const unsigned short* x = (const unsigned short*)d_in[0];
    const unsigned short* wg = (const unsigned short*)d_in[1];
    const unsigned short* wu = (const unsigned short*)d_in[2];
    const unsigned short* wd = (const unsigned short*)d_in[3];
    unsigned short* h = (unsigned short*)d_ws;
    k1<<<dim3(S / 32, F / 32), 64, 0, stream>>>(x, wg, wu, h);
    k2<<<dim3(S / 32, D / 32), 64, 0, stream>>>(h, wd, (unsigned short*)d_out);
}
```

```cpp
#include <hip/hip_runtime.h>
#include <stdint.h>

typedef short bf16x8 __attribute__((ext_vector_type(8)));
typedef short s16x4 __attribute__((ext_vector_type(4)));
typedef float f32x16 __attribute__((ext_vector_type(16)));
typedef float f32x4 __attribute__((ext_vector_type(4)));
typedef float f32x2 __attribute__((ext_vector_type(2)));
typedef __bf16 bf16x2 __attribute__((ext_vector_type(2)));
typedef unsigned u32x4 __attribute__((ext_vector_type(4)));
#define LAS __attribute__((address_space(3)))
#define GAS __attribute__((address_space(1)))
typedef GAS unsigned long long gu64;

extern "C" __device__ unsigned long long glu_dispatch_id() __asm("llvm.amdgcn.dispatch.id");

namespace {

constexpr int S = 2048, D = 448, F = 1792;
constexpr int SLOT = 16384;
constexpr int NWG = 224;
constexpr unsigned H_BYTES = (unsigned)S * F * 2;
constexpr unsigned FLAG_OFF = H_BYTES;
constexpr int PROW = 272;

#define STAMP(i)
#define STAMPR(i)
#define STAMP_DECL
#define STAMP_OUT()

__device__ constexpr int xslot(int s) { return s == 0 ? 0 : s == 1 ? 2 : s == 2 ? 4 : s == 3 ? 6 : s == 4 ? 8 : s == 5 ? 1 : 5; }
__device__ constexpr int wslot(int s) {
    return s == 0 ? 1 : s == 1 ? 3 : s == 2 ? 5 : s == 3 ? 7 : s == 4 ? 9 : s == 5 ? 3 : s == 6 ? 7
         : s == 7 ? 9 : s == 8 ? 3 : s == 9 ? 7 : s == 10 ? 9 : s == 11 ? 0 : s == 12 ? 3 : 2;
}
__device__ constexpr int dslot(int j) {
    return j == 0 ? 7 : j == 1 ? 4 : j == 2 ? 9 : j == 3 ? 6 : j == 4 ? 0 : j == 5 ? 8 : j == 6 ? 3 : j == 7 ? 1
         : j == 8 ? 2 : j == 9 ? 5 : j == 10 ? 7 : j == 11 ? 4 : j == 12 ? 9 : 6;
}
__device__ constexpr int unit_of(int kh, int p) { return 14 * kh + (p < 7 ? 2 * p : 2 * (p - 7) + 1); }

#define WAITV(n) asm volatile("s_waitcnt vmcnt(" #n ")" ::: "memory")
#define BAR() asm volatile("s_barrier" ::: "memory")
#define BAR_LGKM() asm volatile("s_waitcnt lgkmcnt(0)\n\ts_barrier" ::: "memory")
#define SCHED() __builtin_amdgcn_sched_barrier(0)
#define OPAQUE(v) asm volatile("" : "+v"(v))

__device__ __forceinline__ const char LAS* lp(const unsigned (&base)[3], int off) {
    return (const char LAS*)(base[off >> 16] + (unsigned)(off & 0xFFFF));
}
__device__ __forceinline__ bf16x8 lds_b128(const char LAS* p) { return *(const bf16x8 LAS*)p; }
__device__ __forceinline__ s16x4 lds_tr(const char LAS* p) { return __builtin_amdgcn_ds_read_tr16_b64_v4i16((s16x4 LAS*)p); }
__device__ __forceinline__ bf16x8 cat(s16x4 a, s16x4 b) { return __builtin_shufflevector(a, b, 0, 1, 2, 3, 4, 5, 6, 7); }
__device__ __forceinline__ f32x16 mfma(bf16x8 a, bf16x8 b, f32x16 c) {
    return __builtin_amdgcn_mfma_f32_32x32x16_bf16(a, b, c, 0, 0, 0);
}
__device__ __forceinline__ unsigned pack_bf16(float a, float b) {
    f32x2 v = {a, b};
    bf16x2 r = __builtin_convertvector(v, bf16x2);
    return __builtin_bit_cast(unsigned, r);
}
constexpr float NLOG2E = -1.4426950408889634f;
__device__ __forceinline__ float glu(float g, float u) {
    float e = __builtin_amdgcn_exp2f(g * NLOG2E);
    return u * __builtin_amdgcn_rcpf(1.0f + e);
}
__device__ __forceinline__ u32x4 glu_frag(const f32x16& a) {
    u32x4 r;
    r.x = pack_bf16(glu(a[0], a[8]), glu(a[1], a[9]));
    r.y = pack_bf16(glu(a[2], a[10]), glu(a[3], a[11]));
    r.z = pack_bf16(glu(a[4], a[12]), glu(a[5], a[13]));
    r.w = pack_bf16(glu(a[6], a[14]), glu(a[7], a[15]));
    return r;
}
__device__ __forceinline__ void wait_flags(gu64* lines, int lane, unsigned long long tag) {
    const int l = lane & 15;
    gu64* p = lines + ((lane >> 4) & 1) * 16 + (l < 7 ? l : 6);
#pragma unroll 1
    for (unsigned spins = 0; spins < (1u << 22); ++spins) {
        unsigned long long v = __hip_atomic_load(p, __ATOMIC_RELAXED, __HIP_MEMORY_SCOPE_AGENT);
        if (__all(v == tag)) break;
        __builtin_amdgcn_s_sleep(1);
    }
    asm volatile("" ::: "memory");
}

__global__ __launch_bounds__(512, 2) void glu_fused(const void* __restrict__ xp, const void* __restrict__ wgp,
                                                    const void* __restrict__ wup, const void* __restrict__ wdp,
                                                    void* __restrict__ outp, void* __restrict__ ws) {
    __shared__ __attribute__((aligned(1024))) char lds_raw[10 * SLOT];
    char LAS* lds = (char LAS*)lds_raw;
    const int tid = threadIdx.x, lane = tid & 63;
    const int wave = __builtin_amdgcn_readfirstlane(tid >> 6);
    const int b = blockIdx.x, xcd = b & 7, sl = b >> 3;
    const int q7 = sl / 7, r7 = sl - 7 * q7;
    const int rb = 4 * (xcd >> 1) + q7;
    const int cb = 7 * (xcd & 1) + r7;
    const int mb = 4 * xcd + q7;
    const int nb = r7;
    STAMP_DECL; STAMPR(62); STAMP(0);

    if (wave >= 4) {
        const int w = wave - 4, r = lane >> 3, p = lane & 7;
        const __amdgpu_buffer_rsrc_t rx = __builtin_amdgcn_make_buffer_rsrc((void*)xp, 0, S * D * 2, 0x00020000);
        const __amdgpu_buffer_rsrc_t rw = __builtin_amdgcn_make_buffer_rsrc((void*)((w & 1) ? wup : wgp), 0, D * F * 2, 0x00020000);
        const int vx = r * (D * 2) + ((p ^ (((w & 1) << 2) | (r >> 1))) << 4);
        const int vw = r * (F * 2) + ((((p >> 1) ^ (((r >> 1) & 1) | ((w & 1) << 1))) << 5) | ((p & 1) << 4));
        const int sx = (rb * 128 + 8 * w) * (D * 2);
        const int sw = (8 * (w >> 1)) * (F * 2) + cb * 256;
        char LAS* lw = lds + w * 1024;
        auto ix = [&](int s) {
#pragma unroll
            for (int i = 0; i < 4; ++i)
                __builtin_amdgcn_raw_ptr_buffer_load_lds(rx, lw + xslot(s) * SLOT + i * 4096, 16, vx, sx + i * 32 * (D * 2) + s * 128, 0, 0);
        };
        auto iw = [&](int s) {
#pragma unroll
            for (int i = 0; i < 4; ++i)
                __builtin_amdgcn_raw_ptr_buffer_load_lds(rw, lw + wslot(s) * SLOT + i * 4096, 16, vw,
                                                         sw + ((s % 7) * 64 + i * 16) * (F * 2) + (s / 7) * 128, 0, 0);
        };
        ix(0); iw(0); ix(1); iw(1); ix(2); iw(2); ix(3); iw(3); ix(4); iw(4);
        STAMP(1);
        WAITV(32); STAMP(2); BAR(); STAMP(3);
        WAITV(24); STAMP(4); BAR(); STAMP(5); ix(5);
        WAITV(20); STAMP(6); BAR(); STAMP(7); iw(5);
        WAITV(16); STAMP(8); BAR(); STAMP(9); ix(6);
        WAITV(12); STAMP(10); BAR(); STAMP(11); iw(6);
        WAITV(8); STAMP(12); BAR(); STAMP(13); iw(7);
        WAITV(4); STAMP(14); BAR(); STAMP(15); iw(8);
        WAITV(4); STAMP(16); BAR(); STAMP(17); iw(9);
        WAITV(4); STAMP(18); BAR(); STAMP(19); iw(10); iw(11);
        WAITV(8); STAMP(20); BAR(); STAMP(21); iw(12); iw(13);
        if (w < 2) {
            const __amdgpu_buffer_rsrc_t rd = __builtin_amdgcn_make_buffer_rsrc((void*)wdp, 0, F * D * 2, 0x00020000);
            const int vd = r * (D * 2) + ((p ^ (((r >> 1) & 1) << 2)) << 4);
            const int sd = (8 * w) * (D * 2) + nb * 128;
            auto id = [&](int j) {
#pragma unroll
                for (int i = 0; i < 8; ++i)
                    __builtin_amdgcn_raw_ptr_buffer_load_lds(rd, lw + dslot(j) * SLOT + i * 2048, 16, vd,
                                                             sd + (unit_of(i >> 2, j) * 64 + (i & 3) * 16) * (D * 2), 0, 0);
            };
            WAITV(12); STAMP(22); BAR(); STAMP(23); id(0); id(1);
            WAITV(24); STAMP(24); BAR(); STAMP(25); id(2); id(3);
            WAITV(36); STAMP(26); BAR(); STAMP(27); id(4); id(5);
            WAITV(48); STAMP(28); BAR(); STAMP(29); id(6); id(7);
            WAITV(56); STAMP(30); BAR(); STAMP(31); id(8); id(9);
            WAITV(63); STAMP(32); BAR(); STAMP(33); id(10);
            WAITV(63); STAMP(34); BAR(); STAMP(35); id(11);
            WAITV(63); STAMP(36); BAR(); STAMP(37); id(12);
            WAITV(24); STAMP(38); BAR(); STAMP(39); id(13);
            WAITV(0); STAMP(40); BAR(); STAMP(41);
        } else {
            const unsigned long long tag =
                (glu_dispatch_id() + 1ull) * 0x9E3779B97F4A7C15ull ^ ((unsigned long long)__builtin_amdgcn_dispatch_ptr() * 0xD1B54A32D192ED03ull);
            gu64* flags = (gu64*)((char*)ws + FLAG_OFF);
            gu64* mine = flags + (rb * 2 + (cb >= 7)) * 16 + (cb >= 7 ? cb - 7 : cb);
            gu64* theirs = flags + (mb >> 1) * 32;
            WAITV(12); STAMP(22); BAR(); STAMP(23);
            WAITV(8); STAMP(24); BAR(); STAMP(25);
            WAITV(4); STAMP(26); BAR(); STAMP(27);
            WAITV(0); STAMP(28); BAR(); STAMP(29);
            if (w == 3) { if (lane == 0) __hip_atomic_store(mine, tag, __ATOMIC_RELAXED, __HIP_MEMORY_SCOPE_AGENT); STAMP(50); }
            else { wait_flags(theirs, lane, tag); STAMP(50); }
            BAR();
            BAR();
            BAR();
            if (w == 3) { if (lane == 0) __hip_atomic_store(mine + 512, tag, __ATOMIC_RELAXED, __HIP_MEMORY_SCOPE_AGENT); STAMP(51); }
            BAR();
            if (w == 2) { wait_flags(theirs + 512, lane, tag); STAMP(51); }
            BAR();
            BAR();
        }
    } else {
        __builtin_amdgcn_s_setprio(2);
        const int hh = lane >> 5, blk = (lane >> 4) & 1, q = (lane & 15) >> 2, pp = lane & 3;
        const unsigned lbase = (unsigned)(size_t)lds;
        const __amdgpu_buffer_rsrc_t rh = __builtin_amdgcn_make_buffer_rsrc(ws, 0, H_BYTES, 0x00020000);
        f32x16 acc2[2];
        {
            const int sw = wave & 1, fw = wave >> 1;
            unsigned ax[4][3], aw[2][3];
#pragma unroll
            for (int ks = 0; ks < 4; ++ks)
#pragma unroll
                for (int k = 0; k < 3; ++k) {
                    ax[ks][k] = lbase + k * 65536 + sw * 8192 + (lane & 31) * 128 + (((2 * ks + hh) ^ ((lane >> 1) & 7)) << 4);
                    OPAQUE(ax[ks][k]);
                }
#pragma unroll
            for (int t = 0; t < 2; ++t)
#pragma unroll
                for (int k = 0; k < 3; ++k) {
                    aw[t][k] = lbase + k * 65536 + hh * 2048 + blk * 1024 + q * 128 + pp * 8 + (((2 * fw + t) ^ ((q >> 1) | (blk << 1))) << 5);
                    OPAQUE(aw[t][k]);
                }

            f32x16 acc[2][2][2] = {};
            bf16x8 fa[2][2], fb[2][2];
            auto rd0 = [&](int buf, int s, int ks) {
                const int xo = xslot(s % 7) * SLOT, wo = wslot(s) * SLOT + ks * 4096;
                fa[buf][0] = cat(lds_tr(lp(aw[0], wo)), lds_tr(lp(aw[0], wo + 512)));
                fb[buf][0] = lds_b128(lp(ax[ks], xo));
            };
            auto rd1 = [&](int buf, int s, int ks) {
                const int xo = xslot(s % 7) * SLOT, wo = wslot(s) * SLOT + ks * 4096;
                fb[buf][1] = lds_b128(lp(ax[ks], xo + 4096));
                fa[buf][1] = cat(lds_tr(lp(aw[1], wo)), lds_tr(lp(aw[1], wo + 512)));
            };
            auto hstore = [&](int pass, int st, int t, u32x4 v) {
                const int stile = 4 * rb + 2 * sw + st, kstep = 8 * cb + 4 * pass + 2 * fw + t;
                __builtin_amdgcn_raw_buffer_store_b128(v, rh, lane * 16, (stile * 112 + kstep) * 1024, 16);
            };

            BAR();
            STAMP(2);
            rd0(0, 0, 0); rd1(0, 0, 0);
            u32x4 hv;
#pragma unroll
            for (int s = 0; s < 14; ++s) {
                const int pass = s / 7;
#pragma unroll
                for (int ks = 0; ks < 4; ++ks) {
                    const int cur = ks & 1, nxt = cur ^ 1;
                    const int ns = ks == 3 ? s + 1 : s, nks = (ks + 1) & 3;
                    const bool rdn = ns < 14;
                    const int n = (s - 7) * 4 + ks;
                    const bool ep = s >= 7 && s < 11;
                    const int est = (n >> 3) & 1, et = (n >> 2) & 1, e0 = 2 * (n & 3);
                    float t0, t1, x0, x1, r0, r1;
                    if (ks == 3) {
                        if (s == 12) WAITV(0);
                        BAR_LGKM();
                    }
                    SCHED();
                    acc[pass][0][0] = mfma(fa[cur][0], fb[cur][0], acc[pass][0][0]);
                    SCHED();
                    if (rdn) rd0(nxt, ns, nks);
                    if (ep) { t0 = acc[0][est][et][e0] * NLOG2E; OPAQUE(t0); t1 = acc[0][est][et][e0 + 1] * NLOG2E; OPAQUE(t1); }
                    SCHED();
                    acc[pass][1][0] = mfma(fa[cur][0], fb[cur][1], acc[pass][1][0]);
                    SCHED();
                    if (rdn) rd1(nxt, ns, nks);
                    if (ep) { x0 = __builtin_amdgcn_exp2f(t0); OPAQUE(x0); }
                    SCHED();
                    acc[pass][0][1] = mfma(fa[cur][1], fb[cur][0], acc[pass][0][1]);
                    SCHED();
                    if (ep) { x1 = __builtin_amdgcn_exp2f(t1); x0 = 1.0f + x0; OPAQUE(x0); x1 = 1.0f + x1; OPAQUE(x1); r0 = __builtin_amdgcn_rcpf(x0); OPAQUE(r0); }
                    SCHED();
                    acc[pass][1][1] = mfma(fa[cur][1], fb[cur][1], acc[pass][1][1]);
                    SCHED();
                    if (ep) {
                        r1 = __builtin_amdgcn_rcpf(x1);
                        r0 = r0 * acc[0][est][et][e0 + 8]; OPAQUE(r0);
                        r1 = r1 * acc[0][est][et][e0 + 9]; OPAQUE(r1);
                        hv[n & 3] = pack_bf16(r0, r1);
                        if ((n & 3) == 3) hstore(0, est, et, hv);
                        SCHED();
                    }
                }
                if (s == 6) { STAMP(3); }
            }
            STAMP(4);

            const int kh = wave >> 1, st2 = wave & 1;
            const int hbase = ((2 * mb + st2) * 112 + kh * 56) * 1024;
            u32x4 ring[28];
            auto hload = [&](int slot, int p, int ks) {
                const int u = (p < 7 ? 2 * p : 2 * (p - 7) + 1);
                ring[slot] = __builtin_amdgcn_raw_buffer_load_b128(rh, lane * 16, hbase + (u * 4 + ks) * 1024, 16);
            };
#pragma unroll
            for (int j = 0; j < 12; ++j) hload(j, j >> 2, j & 3);
            SCHED();
#pragma unroll
            for (int st = 0; st < 2; ++st)
#pragma unroll
                for (int t = 0; t < 2; ++t) hstore(1, st, t, glu_frag(acc[1][st][t]));
            SCHED();
#pragma unroll
            for (int j = 12; j < 28; ++j) hload(j, j >> 2, j & 3);
            SCHED();
            STAMP(5);

            unsigned ad[2][3];
#pragma unroll
            for (int nt = 0; nt < 2; ++nt)
#pragma unroll
                for (int k = 0; k < 3; ++k) {
                    ad[nt][k] = lbase + k * 65536 + kh * 8192 + (4 * hh + q) * 128 + ((nt ^ (q >> 1)) << 6) + blk * 32 + pp * 8;
                    OPAQUE(ad[nt][k]);
                }
            bf16x8 fd[2][2];
            auto rdd = [&](int buf, int nt, int p, int ks) {
                const int o = dslot(p) * SLOT + ks * 2048;
                fd[buf][nt] = cat(lds_tr(lp(ad[nt], o)), lds_tr(lp(ad[nt], o + 1024)));
            };
            acc2[0] = f32x16{}; acc2[1] = f32x16{};
            rdd(0, 0, 0, 0); rdd(0, 1, 0, 0);
#pragma unroll
            for (int p = 0; p < 14; ++p) {
                if (p == 4) {
                    STAMP(6);
#pragma unroll
                    for (int j = 0; j < 16; ++j) hload(j, 7 + (j >> 2), j & 3);
                }
                if (p == 7) STAMP(7);
#pragma unroll
                for (int ks = 0; ks < 4; ++ks) {
                    const int cur = ks & 1, nxt = cur ^ 1, n = 4 * p + ks;
                    const int np = ks == 3 ? p + 1 : p, nks = (ks + 1) & 3;
                    if (ks == 3) {
                        if (p == 1) WAITV(16);
                        if (p <= 3 || p == 9) BAR_LGKM();
                    }
                    const bf16x8 hb = __builtin_bit_cast(bf16x8, ring[n % 28]);
                    SCHED();
                    acc2[0] = mfma(fd[cur][0], hb, acc2[0]);
                    SCHED();
                    if (np < 14) rdd(nxt, 0, np, nks);
                    SCHED();
                    acc2[1] = mfma(fd[cur][1], hb, acc2[1]);
                    SCHED();
                    if (np < 14) rdd(nxt, 1, np, nks);
                    if (n >= 16 && n < 28) hload(n, 7 + (n >> 2), n & 3);
                    SCHED();
                }
            }
            STAMP(8);
            char LAS* pw = lds + kh * (64 * PROW) + (st2 * 32 + (lane & 31)) * PROW + hh * 16;
#pragma unroll
            for (int nt = 0; nt < 2; ++nt)
#pragma unroll
                for (int g = 0; g < 4; ++g) {
                    f32x4 v = {acc2[nt][4 * g], acc2[nt][4 * g + 1], acc2[nt][4 * g + 2], acc2[nt][4 * g + 3]};
                    *(f32x4 LAS*)(pw + nt * 128 + g * 32) = v;
                }
        }
    }
    BAR_LGKM();
    {
        const int row = tid >> 3, seg = tid & 7;
        const char LAS* pr = lds + row * PROW + seg * 32;
        const f32x4 a0 = *(const f32x4 LAS*)(pr), a1 = *(const f32x4 LAS*)(pr + 16);
        const f32x4 b0 = *(const f32x4 LAS*)(pr + 64 * PROW), b1 = *(const f32x4 LAS*)(pr + 64 * PROW + 16);
        u32x4 o;
        o.x = pack_bf16(a0[0] + b0[0], a0[1] + b0[1]);
        o.y = pack_bf16(a0[2] + b0[2], a0[3] + b0[3]);
        o.z = pack_bf16(a1[0] + b1[0], a1[1] + b1[1]);
        o.w = pack_bf16(a1[2] + b1[2], a1[3] + b1[3]);
        *(u32x4 GAS*)((char GAS*)outp + (size_t)(mb * 64 + row) * (D * 2) + nb * 128 + seg * 16) = o;
    }
    STAMP(61); STAMPR(63); STAMP_OUT();
}

}

extern "C" void kernel_launch(void* const* d_in, const int* in_sizes, int n_in, void* d_out, int out_size, void* d_ws,
                              size_t ws_size, hipStream_t stream) {
    (void)in_sizes; (void)n_in; (void)out_size; (void)ws_size;
    glu_fused<<<NWG, 512, 0, stream>>>(d_in[0], d_in[1], d_in[2], d_in[3], d_out, d_ws);
}
```

```cpp
#include <hip/hip_runtime.h>
#include <stdint.h>

typedef short bf16x8 __attribute__((ext_vector_type(8)));
typedef short s16x4 __attribute__((ext_vector_type(4)));
typedef float f32x16 __attribute__((ext_vector_type(16)));
typedef float f32x4 __attribute__((ext_vector_type(4)));
typedef float f32x2 __attribute__((ext_vector_type(2)));
typedef __bf16 bf16x2 __attribute__((ext_vector_type(2)));
typedef unsigned u32x4 __attribute__((ext_vector_type(4)));
#define LAS __attribute__((address_space(3)))
#define GAS __attribute__((address_space(1)))
typedef GAS unsigned long long gu64;

extern "C" __device__ unsigned long long glu_dispatch_id() __asm("llvm.amdgcn.dispatch.id");

namespace {

constexpr int S = 2048, D = 448, F = 1792;
constexpr int SLOT = 16384;
constexpr int NWG = 224;
constexpr unsigned H_BYTES = (unsigned)S * F * 2;
constexpr unsigned FLAG_OFF = H_BYTES;
constexpr int PROW = 272;

#define STAMP(i)
#define STAMPR(i)
#define STAMP_DECL
#define STAMP_OUT()

__device__ constexpr int xslot(int s) { return s == 0 ? 0 : s == 1 ? 2 : s == 2 ? 4 : s == 3 ? 6 : s == 4 ? 8 : s == 5 ? 1 : 5; }
__device__ constexpr int wslot(int s) {
    return s == 0 ? 1 : s == 1 ? 3 : s == 2 ? 5 : s == 3 ? 7 : s == 4 ? 9 : s == 5 ? 3 : s == 6 ? 7
         : s == 7 ? 9 : s == 8 ? 3 : s == 9 ? 7 : s == 10 ? 9 : s == 11 ? 0 : s == 12 ? 3 : 2;
}
__device__ constexpr int dslot(int j) {
    return j == 0 ? 7 : j == 1 ? 4 : j == 2 ? 9 : j == 3 ? 6 : j == 4 ? 0 : j == 5 ? 8 : j == 6 ? 3 : j == 7 ? 1
         : j == 8 ? 2 : j == 9 ? 5 : j == 10 ? 7 : j == 11 ? 4 : j == 12 ? 9 : 6;
}
__device__ constexpr int unit_of(int kh, int p) { return 14 * kh + (p < 7 ? 2 * p : 2 * (p - 7) + 1); }

#define WAITV(n) asm volatile("s_waitcnt vmcnt(" #n ")" ::: "memory")
#define BAR() asm volatile("s_barrier" ::: "memory")
#define BAR_LGKM() asm volatile("s_waitcnt lgkmcnt(0)\n\ts_barrier" ::: "memory")
#define SCHED() __builtin_amdgcn_sched_barrier(0)
#define OPAQUE(v) asm volatile("" : "+v"(v))

__device__ __forceinline__ const char LAS* lp(const unsigned (&base)[3], int off) {
    return (const char LAS*)(base[off >> 16] + (unsigned)(off & 0xFFFF));
}
__device__ __forceinline__ bf16x8 lds_b128(const char LAS* p) { return *(const bf16x8 LAS*)p; }
__device__ __forceinline__ s16x4 lds_tr(const char LAS* p) { return __builtin_amdgcn_ds_read_tr16_b64_v4i16((s16x4 LAS*)p); }
__device__ __forceinline__ bf16x8 cat(s16x4 a, s16x4 b) { return __builtin_shufflevector(a, b, 0, 1, 2, 3, 4, 5, 6, 7); }
__device__ __forceinline__ f32x16 mfma(bf16x8 a, bf16x8 b, f32x16 c) {
    return __builtin_amdgcn_mfma_f32_32x32x16_bf16(a, b, c, 0, 0, 0);
}
__device__ __forceinline__ unsigned pack_bf16(float a, float b) {
    f32x2 v = {a, b};
    bf16x2 r = __builtin_convertvector(v, bf16x2);
    return __builtin_bit_cast(unsigned, r);
}
constexpr float NLOG2E = -1.4426950408889634f;
__device__ __forceinline__ float glu(float g, float u) {
    float e = __builtin_amdgcn_exp2f(g * NLOG2E);
    return u * __builtin_amdgcn_rcpf(1.0f + e);
}
__device__ __forceinline__ u32x4 glu_frag(const f32x16& a) {
    u32x4 r;
    r.x = pack_bf16(glu(a[0], a[8]), glu(a[1], a[9]));
    r.y = pack_bf16(glu(a[2], a[10]), glu(a[3], a[11]));
    r.z = pack_bf16(glu(a[4], a[12]), glu(a[5], a[13]));
    r.w = pack_bf16(glu(a[6], a[14]), glu(a[7], a[15]));
    return r;
}
__device__ __forceinline__ void wait_flags(gu64* lines, int lane, unsigned long long tag) {
    const int l = lane & 15;
    gu64* p = lines + ((lane >> 4) & 1) * 16 + (l < 7 ? l : 6);
#pragma unroll 1
    for (unsigned spins = 0; spins < (1u << 22); ++spins) {
        unsigned long long v = __hip_atomic_load(p, __ATOMIC_RELAXED, __HIP_MEMORY_SCOPE_AGENT);
        if (__all(v == tag)) break;
        __builtin_amdgcn_s_sleep(1);
    }
    asm volatile("" ::: "memory");
}

__global__ __launch_bounds__(512, 2) void glu_fused(const void* __restrict__ xp, const void* __restrict__ wgp,
                                                    const void* __restrict__ wup, const void* __restrict__ wdp,
                                                    void* __restrict__ outp, void* __restrict__ ws) {
    __shared__ __attribute__((aligned(1024))) char lds_raw[10 * SLOT];
    char LAS* lds = (char LAS*)lds_raw;
    const int tid = threadIdx.x, lane = tid & 63;
    const int wave = __builtin_amdgcn_readfirstlane(tid >> 6);
    const int b = blockIdx.x, xcd = b & 7, sl = b >> 3;
    const int q7 = sl / 7, r7 = sl - 7 * q7;
    const int rb = 4 * (xcd >> 1) + q7;
    const int cb = 7 * (xcd & 1) + r7;
    const int mb = 4 * xcd + q7;
    const int nb = r7;

    if (blockIdx.x == NWG) {
        unsigned t0_, d_ = 0;
        unsigned long long k0_, k1_;
        asm volatile("s_memtime %0" : "=s"(k0_));
        asm volatile("s_waitcnt lgkmcnt(0)" :: "s"(wdp) : "memory");
        asm volatile("s_memtime %0\n\ts_waitcnt lgkmcnt(0)" : "=s"(k1_) :: "memory");
        t0_ = (unsigned)k0_; d_ = (unsigned)(k1_ - k0_);
        d_ *= 64u;
        if (wave == 0) { while ((unsigned)__builtin_amdgcn_s_memtime() - t0_ < d_) __builtin_amdgcn_s_sleep(4); }
        return;
    }

    if (wave >= 4) {
        const int w = wave - 4, r = lane >> 3, p = lane & 7;
        const __amdgpu_buffer_rsrc_t rx = __builtin_amdgcn_make_buffer_rsrc((void*)xp, 0, S * D * 2, 0x00020000);
        const __amdgpu_buffer_rsrc_t rw = __builtin_amdgcn_make_buffer_rsrc((void*)((w & 1) ? wup : wgp), 0, D * F * 2, 0x00020000);
        const int vx = r * (D * 2) + ((p ^ (((w & 1) << 2) | (r >> 1))) << 4);
        const int vw = r * (F * 2) + ((((p >> 1) ^ (((r >> 1) & 1) | ((w & 1) << 1))) << 5) | ((p & 1) << 4));
        const int sx = (rb * 128 + 8 * w) * (D * 2);
        const int sw = (8 * (w >> 1)) * (F * 2) + cb * 256;
        char LAS* lw = lds + w * 1024;
        auto ix = [&](int s) {
#pragma unroll
            for (int i = 0; i < 4; ++i)
                __builtin_amdgcn_raw_ptr_buffer_load_lds(rx, lw + xslot(s) * SLOT + i * 4096, 16, vx, sx + i * 32 * (D * 2) + s * 128, 0, 0);
        };
        auto iw = [&](int s) {
#pragma unroll
            for (int i = 0; i < 4; ++i)
                __builtin_amdgcn_raw_ptr_buffer_load_lds(rw, lw + wslot(s) * SLOT + i * 4096, 16, vw,
                                                         sw + ((s % 7) * 64 + i * 16) * (F * 2) + (s / 7) * 128, 0, 0);
        };
        ix(0); iw(0); ix(1); iw(1); ix(2); iw(2); ix(3); iw(3); ix(4); iw(4);
        STAMP(1);
        WAITV(32); STAMP(2); BAR(); STAMP(3);
        WAITV(24); STAMP(4); BAR(); STAMP(5); ix(5);
        WAITV(20); STAMP(6); BAR(); STAMP(7); iw(5);
        WAITV(16); STAMP(8); BAR(); STAMP(9); ix(6);
        WAITV(12); STAMP(10); BAR(); STAMP(11); iw(6);
        WAITV(8); STAMP(12); BAR(); STAMP(13); iw(7);
        WAITV(4); STAMP(14); BAR(); STAMP(15); iw(8);
        WAITV(4); STAMP(16); BAR(); STAMP(17); iw(9);
        WAITV(4); STAMP(18); BAR(); STAMP(19); iw(10); iw(11);
        WAITV(8); STAMP(20); BAR(); STAMP(21); iw(12); iw(13);
        if (w < 2) {
            const __amdgpu_buffer_rsrc_t rd = __builtin_amdgcn_make_buffer_rsrc((void*)wdp, 0, F * D * 2, 0x00020000);
            const int vd = r * (D * 2) + ((p ^ (((r >> 1) & 1) << 2)) << 4);
            const int sd = (8 * w) * (D * 2) + nb * 128;
            auto id = [&](int j) {
#pragma unroll
                for (int i = 0; i < 8; ++i)
                    __builtin_amdgcn_raw_ptr_buffer_load_lds(rd, lw + dslot(j) * SLOT + i * 2048, 16, vd,
                                                             sd + (unit_of(i >> 2, j) * 64 + (i & 3) * 16) * (D * 2), 0, 0);
            };
            WAITV(12); STAMP(22); BAR(); STAMP(23); id(0); id(1);
            WAITV(24); STAMP(24); BAR(); STAMP(25); id(2); id(3);
            WAITV(36); STAMP(26); BAR(); STAMP(27); id(4); id(5);
            WAITV(48); STAMP(28); BAR(); STAMP(29); id(6); id(7);
            WAITV(56); STAMP(30); BAR(); STAMP(31); id(8); id(9);
            WAITV(63); STAMP(32); BAR(); STAMP(33); id(10);
            WAITV(63); STAMP(34); BAR(); STAMP(35); id(11);
            WAITV(63); STAMP(36); BAR(); STAMP(37); id(12);
            WAITV(24); STAMP(38); BAR(); STAMP(39); id(13);
            WAITV(0); STAMP(40); BAR(); STAMP(41);
        } else {
            const unsigned long long tag =
                (glu_dispatch_id() + 1ull) * 0x9E3779B97F4A7C15ull ^ ((unsigned long long)__builtin_amdgcn_dispatch_ptr() * 0xD1B54A32D192ED03ull);
            gu64* flags = (gu64*)((char*)ws + FLAG_OFF);
            gu64* mine = flags + (rb * 2 + (cb >= 7)) * 16 + (cb >= 7 ? cb - 7 : cb);
            gu64* theirs = flags + (mb >> 1) * 32;
            WAITV(12); STAMP(22); BAR(); STAMP(23);
            WAITV(8); STAMP(24); BAR(); STAMP(25);
            WAITV(4); STAMP(26); BAR(); STAMP(27);
            WAITV(0); STAMP(28); BAR(); STAMP(29);
            if (w == 3) { if (lane == 0) __hip_atomic_store(mine, tag, __ATOMIC_RELAXED, __HIP_MEMORY_SCOPE_AGENT); STAMP(50); }
            else { wait_flags(theirs, lane, tag); STAMP(50); }
            BAR();
            BAR();
            BAR();
            if (w == 3) { if (lane == 0) __hip_atomic_store(mine + 512, tag, __ATOMIC_RELAXED, __HIP_MEMORY_SCOPE_AGENT); STAMP(51); }
            BAR();
            if (w == 2) { wait_flags(theirs + 512, lane, tag); STAMP(51); }
            BAR();
            BAR();
        }
    } else {
        __builtin_amdgcn_s_setprio(2);
        const int hh = lane >> 5, blk = (lane >> 4) & 1, q = (lane & 15) >> 2, pp = lane & 3;
        const unsigned lbase = (unsigned)(size_t)lds;
        const __amdgpu_buffer_rsrc_t rh = __builtin_amdgcn_make_buffer_rsrc(ws, 0, H_BYTES, 0x00020000);
        f32x16 acc2[2];
        {
            const int sw = wave & 1, fw = wave >> 1;
            unsigned ax[4][3], aw[2][3];
#pragma unroll
            for (int ks = 0; ks < 4; ++ks)
#pragma unroll
                for (int k = 0; k < 3; ++k) {
                    ax[ks][k] = lbase + k * 65536 + sw * 8192 + (lane & 31) * 128 + (((2 * ks + hh) ^ ((lane >> 1) & 7)) << 4);
                    OPAQUE(ax[ks][k]);
                }
#pragma unroll
            for (int t = 0; t < 2; ++t)
#pragma unroll
                for (int k = 0; k < 3; ++k) {
                    aw[t][k] = lbase + k * 65536 + hh * 2048 + blk * 1024 + q * 128 + pp * 8 + (((2 * fw + t) ^ ((q >> 1) | (blk << 1))) << 5);
                    OPAQUE(aw[t][k]);
                }

            f32x16 acc[2][2][2] = {};
            bf16x8 fa[2][2], fb[2][2];
            auto rd0 = [&](int buf, int s, int ks) {
                const int xo = xslot(s % 7) * SLOT, wo = wslot(s) * SLOT + ks * 4096;
                fa[buf][0] = cat(lds_tr(lp(aw[0], wo)), lds_tr(lp(aw[0], wo + 512)));
                fb[buf][0] = lds_b128(lp(ax[ks], xo));
            };
            auto rd1 = [&](int buf, int s, int ks) {
                const int xo = xslot(s % 7) * SLOT, wo = wslot(s) * SLOT + ks * 4096;
                fb[buf][1] = lds_b128(lp(ax[ks], xo + 4096));
                fa[buf][1] = cat(lds_tr(lp(aw[1], wo)), lds_tr(lp(aw[1], wo + 512)));
            };
            auto hstore = [&](int pass, int st, int t, u32x4 v) {
                const int stile = 4 * rb + 2 * sw + st, kstep = 8 * cb + 4 * pass + 2 * fw + t;
                __builtin_amdgcn_raw_buffer_store_b128(v, rh, lane * 16, (stile * 112 + kstep) * 1024, 16);
            };

            BAR();
            STAMP(2);
            rd0(0, 0, 0); rd1(0, 0, 0);
            u32x4 hv;
#pragma unroll
            for (int s = 0; s < 14; ++s) {
                const int pass = s / 7;
#pragma unroll
                for (int ks = 0; ks < 4; ++ks) {
                    const int cur = ks & 1, nxt = cur ^ 1;
                    const int ns = ks == 3 ? s + 1 : s, nks = (ks + 1) & 3;
                    const bool rdn = ns < 14;
                    const int n = (s - 7) * 4 + ks;
                    const bool ep = s >= 7 && s < 11;
                    const int est = (n >> 3) & 1, et = (n >> 2) & 1, e0 = 2 * (n & 3);
                    float t0, t1, x0, x1, r0, r1;
                    if (ks == 3) {
                        if (s == 12) WAITV(0);
                        BAR_LGKM();
                    }
                    SCHED();
                    acc[pass][0][0] = mfma(fa[cur][0], fb[cur][0], acc[pass][0][0]);
                    SCHED();
                    if (rdn) rd0(nxt, ns, nks);
                    if (ep) { t0 = acc[0][est][et][e0] * NLOG2E; OPAQUE(t0); t1 = acc[0][est][et][e0 + 1] * NLOG2E; OPAQUE(t1); }
                    SCHED();
                    acc[pass][1][0] = mfma(fa[cur][0], fb[cur][1], acc[pass][1][0]);
                    SCHED();
                    if (rdn) rd1(nxt, ns, nks);
                    if (ep) { x0 = __builtin_amdgcn_exp2f(t0); OPAQUE(x0); }
                    SCHED();
                    acc[pass][0][1] = mfma(fa[cur][1], fb[cur][0], acc[pass][0][1]);
                    SCHED();
                    if (ep) { x1 = __builtin_amdgcn_exp2f(t1); x0 = 1.0f + x0; OPAQUE(x0); x1 = 1.0f + x1; OPAQUE(x1); r0 = __builtin_amdgcn_rcpf(x0); OPAQUE(r0); }
                    SCHED();
                    acc[pass][1][1] = mfma(fa[cur][1], fb[cur][1], acc[pass][1][1]);
                    SCHED();
                    if (ep) {
                        r1 = __builtin_amdgcn_rcpf(x1);
                        r0 = r0 * acc[0][est][et][e0 + 8]; OPAQUE(r0);
                        r1 = r1 * acc[0][est][et][e0 + 9]; OPAQUE(r1);
                        hv[n & 3] = pack_bf16(r0, r1);
                        if ((n & 3) == 3) hstore(0, est, et, hv);
                        SCHED();
                    }
                }
                if (s == 6) { STAMP(3); }
            }
            STAMP(4);

            const int kh = wave >> 1, st2 = wave & 1;
            const int hbase = ((2 * mb + st2) * 112 + kh * 56) * 1024;
            u32x4 ring[28];
            auto hload = [&](int slot, int p, int ks) {
                const int u = (p < 7 ? 2 * p : 2 * (p - 7) + 1);
                ring[slot] = __builtin_amdgcn_raw_buffer_load_b128(rh, lane * 16, hbase + (u * 4 + ks) * 1024, 16);
            };
#pragma unroll
            for (int j = 0; j < 12; ++j) hload(j, j >> 2, j & 3);
            SCHED();
#pragma unroll
            for (int st = 0; st < 2; ++st)
#pragma unroll
                for (int t = 0; t < 2; ++t) hstore(1, st, t, glu_frag(acc[1][st][t]));
            SCHED();
#pragma unroll
            for (int j = 12; j < 28; ++j) hload(j, j >> 2, j & 3);
            SCHED();
            STAMP(5);

            unsigned ad[2][3];
#pragma unroll
            for (int nt = 0; nt < 2; ++nt)
#pragma unroll
                for (int k = 0; k < 3; ++k) {
                    ad[nt][k] = lbase + k * 65536 + kh * 8192 + (4 * hh + q) * 128 + ((nt ^ (q >> 1)) << 6) + blk * 32 + pp * 8;
                    OPAQUE(ad[nt][k]);
                }
            bf16x8 fd[2][2];
            auto rdd = [&](int buf, int nt, int p, int ks) {
                const int o = dslot(p) * SLOT + ks * 2048;
                fd[buf][nt] = cat(lds_tr(lp(ad[nt], o)), lds_tr(lp(ad[nt], o + 1024)));
            };
            acc2[0] = f32x16{}; acc2[1] = f32x16{};
            rdd(0, 0, 0, 0); rdd(0, 1, 0, 0);
#pragma unroll
            for (int p = 0; p < 14; ++p) {
                if (p == 4) {
                    STAMP(6);
#pragma unroll
                    for (int j = 0; j < 16; ++j) hload(j, 7 + (j >> 2), j & 3);
                }
                if (p == 7) STAMP(7);
#pragma unroll
                for (int ks = 0; ks < 4; ++ks) {
                    const int cur = ks & 1, nxt = cur ^ 1, n = 4 * p + ks;
                    const int np = ks == 3 ? p + 1 : p, nks = (ks + 1) & 3;
                    if (ks == 3) {
                        if (p == 1) WAITV(16);
                        if (p <= 3 || p == 9) BAR_LGKM();
                    }
                    const bf16x8 hb = __builtin_bit_cast(bf16x8, ring[n % 28]);
                    SCHED();
                    acc2[0] = mfma(fd[cur][0], hb, acc2[0]);
                    SCHED();
                    if (np < 14) rdd(nxt, 0, np, nks);
                    SCHED();
                    acc2[1] = mfma(fd[cur][1], hb, acc2[1]);
                    SCHED();
                    if (np < 14) rdd(nxt, 1, np, nks);
                    if (n >= 16 && n < 28) hload(n, 7 + (n >> 2), n & 3);
                    SCHED();
                }
            }
            STAMP(8);
            char LAS* pw = lds + kh * (64 * PROW) + (st2 * 32 + (lane & 31)) * PROW + hh * 16;
#pragma unroll
            for (int nt = 0; nt < 2; ++nt)
#pragma unroll
                for (int g = 0; g < 4; ++g) {
                    f32x4 v = {acc2[nt][4 * g], acc2[nt][4 * g + 1], acc2[nt][4 * g + 2], acc2[nt][4 * g + 3]};
                    *(f32x4 LAS*)(pw + nt * 128 + g * 32) = v;
                }
        }
    }
    BAR_LGKM();
    {
        const int row = tid >> 3, seg = tid & 7;
        const char LAS* pr = lds + row * PROW + seg * 32;
        const f32x4 a0 = *(const f32x4 LAS*)(pr), a1 = *(const f32x4 LAS*)(pr + 16);
        const f32x4 b0 = *(const f32x4 LAS*)(pr + 64 * PROW), b1 = *(const f32x4 LAS*)(pr + 64 * PROW + 16);
        u32x4 o;
        o.x = pack_bf16(a0[0] + b0[0], a0[1] + b0[1]);
        o.y = pack_bf16(a0[2] + b0[2], a0[3] + b0[3]);
        o.z = pack_bf16(a1[0] + b1[0], a1[1] + b1[1]);
        o.w = pack_bf16(a1[2] + b1[2], a1[3] + b1[3]);
        *(u32x4 GAS*)((char GAS*)outp + (size_t)(mb * 64 + row) * (D * 2) + nb * 128 + seg * 16) = o;
    }
    STAMP(61); STAMPR(63); STAMP_OUT();
}

}

extern "C" void kernel_launch(void* const* d_in, const int* in_sizes, int n_in, void* d_out, int out_size, void* d_ws,
                              size_t ws_size, hipStream_t stream) {
    (void)in_sizes; (void)n_in; (void)out_size; (void)ws_size;
    glu_fused<<<NWG + 1, 512, 0, stream>>>(d_in[0], d_in[1], d_in[2], d_in[3], d_out, d_ws);
}
```

```cpp
#include <hip/hip_runtime.h>
#include <stdint.h>

typedef short bf16x8 __attribute__((ext_vector_type(8)));
typedef short s16x4 __attribute__((ext_vector_type(4)));
typedef float f32x16 __attribute__((ext_vector_type(16)));
typedef float f32x4 __attribute__((ext_vector_type(4)));
typedef float f32x2 __attribute__((ext_vector_type(2)));
typedef __bf16 bf16x2 __attribute__((ext_vector_type(2)));
typedef unsigned u32x4 __attribute__((ext_vector_type(4)));
#define LAS __attribute__((address_space(3)))
#define GAS __attribute__((address_space(1)))
typedef GAS unsigned long long gu64;

extern "C" __device__ unsigned long long glu_dispatch_id() __asm("llvm.amdgcn.dispatch.id");

#define PUBA 12
#define HELPERS 1
#define ROT 1
#define CODETOUCH 1
#define HELP_DELAY 0

namespace {

constexpr int S = 2048, D = 448, F = 1792;
constexpr int SLOT = 16384;
constexpr int NWG = 224;
constexpr int GRID = HELPERS ? 256 : 224;
constexpr unsigned H_BYTES = (unsigned)S * F * 2;
constexpr unsigned FLAG_OFF = H_BYTES;

#define STAMP(i)
#define STAMPR(i)
#define STAMP_DECL
#define STAMP_OUT()

__device__ constexpr int wslot(int s) { return 7 + s % 3; }
__device__ constexpr int dslot(int part, int j) { return part == 0 ? (j < 6 ? j : 9) : (j == 0 ? 7 : j == 1 ? 8 : j == 2 ? 6 : j - 3); }

#define WAITV(n) asm volatile("s_waitcnt vmcnt(" #n ")" ::: "memory")
#define BAR() asm volatile("s_barrier" ::: "memory")
#define BAR_LGKM() asm volatile("s_waitcnt lgkmcnt(0)\n\ts_barrier" ::: "memory")
#define SCHED() __builtin_amdgcn_sched_barrier(0)
#define OPAQUE(v) asm volatile("" : "+v"(v))

__device__ __forceinline__ const char LAS* lp(const unsigned (&base)[3], int off) {
    return (const char LAS*)(base[off >> 16] + (unsigned)(off & 0xFFFF));
}
__device__ __forceinline__ bf16x8 lds_b128(const char LAS* p) { return *(const bf16x8 LAS*)p; }
__device__ __forceinline__ s16x4 lds_tr(const char LAS* p) { return __builtin_amdgcn_ds_read_tr16_b64_v4i16((s16x4 LAS*)p); }
__device__ __forceinline__ bf16x8 cat(s16x4 a, s16x4 b) { return __builtin_shufflevector(a, b, 0, 1, 2, 3, 4, 5, 6, 7); }
__device__ __forceinline__ f32x16 mfma(bf16x8 a, bf16x8 b, f32x16 c) {
    return __builtin_amdgcn_mfma_f32_32x32x16_bf16(a, b, c, 0, 0, 0);
}
__device__ __forceinline__ unsigned pack_bf16(float a, float b) {
    f32x2 v = {a, b};
    bf16x2 r = __builtin_convertvector(v, bf16x2);
    return __builtin_bit_cast(unsigned, r);
}
constexpr float NLOG2E = -1.4426950408889634f;
__device__ __forceinline__ float glu(float g, float u) {
    float e = __builtin_amdgcn_exp2f(g * NLOG2E);
    return u * __builtin_amdgcn_rcpf(1.0f + e);
}
__device__ __forceinline__ u32x4 glu_frag(const f32x16& a) {
    u32x4 r;
    r.x = pack_bf16(glu(a[0], a[8]), glu(a[1], a[9]));
    r.y = pack_bf16(glu(a[2], a[10]), glu(a[3], a[11]));
    r.z = pack_bf16(glu(a[4], a[12]), glu(a[5], a[13]));
    r.w = pack_bf16(glu(a[6], a[14]), glu(a[7], a[15]));
    return r;
}
__device__ __forceinline__ u32x4 make_rsrc(const void* p, unsigned bytes) {
    const unsigned long long a = (unsigned long long)p;
    u32x4 r = {(unsigned)a, (unsigned)(a >> 32) & 0xFFFFu, bytes, 0x00020000u};
    return r;
}
__device__ __forceinline__ void dma(const u32x4& rs, unsigned ldsaddr, int voff, int soff) {
    asm volatile("s_mov_b32 m0, %0\n\ts_nop 0\n\tbuffer_load_dwordx4 %1, %2, %3 offen lds"
                 :: "s"(ldsaddr), "v"(voff), "s"(rs), "s"(soff) : "memory");
}
__device__ __forceinline__ u32x4 load_sc1(const u32x4& rs, int voff, int soff) {
    u32x4 v;
    asm volatile("buffer_load_dwordx4 %0, %1, %2, %3 offen sc1" : "=v"(v) : "v"(voff), "s"(rs), "s"(soff) : "memory");
    return v;
}
template <int N> __device__ __forceinline__ void wait_for(u32x4& v) { asm volatile("s_waitcnt vmcnt(%1)" : "+v"(v) : "n"(N) : "memory"); }
__device__ __forceinline__ unsigned long long make_tag() {
    return (glu_dispatch_id() + 1ull) * 0x9E3779B97F4A7C15ull ^ ((unsigned long long)__builtin_amdgcn_dispatch_ptr() * 0xD1B54A32D192ED03ull);
}
__device__ __forceinline__ gu64* flag_line(void* ws, int pass, int rb, int half, int kh) {
    return (gu64*)((char*)ws + FLAG_OFF) + (((pass * 16 + rb) * 2 + half) * 2 + kh) * 16;
}

__global__ __launch_bounds__(512, 2) void glu_fused(const void* __restrict__ xp, const void* __restrict__ wgp,
                                                    const void* __restrict__ wup, const void* __restrict__ wdp,
                                                    void* __restrict__ outp, void* __restrict__ ws) {
    __shared__ __attribute__((aligned(1024))) char lds_raw[10 * SLOT];
    char LAS* lds = (char LAS*)lds_raw;
    const int tid = threadIdx.x, lane = tid & 63;
    const int wave = __builtin_amdgcn_readfirstlane(tid >> 6);
    const int b = blockIdx.x, xcd = b & 7, sl = b >> 3;
    STAMP_DECL; STAMPR(62); STAMP(0);
    if (wave == 7) {
        unsigned long long pc; unsigned len;
        asm volatile("s_getpc_b64 %0\n7701:\n\ts_mov_b32 %1, 7702f-7701b" : "=s"(pc), "=s"(len));
        const unsigned off = (unsigned)(sl * 8 + (lane & 7)) * 128u;
        if (lane < 8 && off < len)
            asm volatile("s_mov_b32 m0, %0\n\ts_nop 0\n\tglobal_load_lds_dword %1, %2"
                         :: "s"((unsigned)(size_t)lds + 9 * SLOT + 3 * 1024), "v"(off), "s"(pc) : "memory");
    }
    if (sl >= 28) {
        const int hid = sl - 28;
        if (HELP_DELAY) __builtin_amdgcn_s_sleep(HELP_DELAY);
#pragma unroll
        for (int list = 0; list < 3; ++list) {
#pragma unroll
            for (int it = 0; it < 4; ++it) {
                int i = ((it * 4 + hid) * 8 + wave) * 64 + lane;
                if (i < 6272) {
                    const char* p;
                    if (list == 0) {
                        i += (xcd >> 1) * 1568; i -= i >= 6272 ? 6272 : 0;
                        const int k = i / 14, r = i - 14 * k;
                        p = (const char*)((r & 1) ? wup : wgp) + k * (F * 2) + (7 * (xcd & 1) + (r >> 1)) * 256 + 128;
                    } else {
                        i += xcd * 784; i -= i >= 6272 ? 6272 : 0;
                        const int c = i / 448, r = i - 448 * c;
                        p = (const char*)wdp + (c * 128 + (list - 1) * 64) * (D * 2) + r * 128;
                    }
                    asm volatile("s_mov_b32 m0, %0\n\ts_nop 0\n\tglobal_load_lds_dword %1, off"
                                 :: "s"((unsigned)(size_t)lds + wave * 256), "v"(p) : "memory");
                }
            }
        }
        WAITV(0);
        STAMP(61); STAMPR(63); STAMP_OUT();
        return;
    }
    const int q7 = sl / 7, r7 = sl - 7 * q7;
    const int rb = 4 * (xcd >> 1) + q7;
    const int cb = 7 * (xcd & 1) + r7;
    const int mb = 4 * xcd + q7;
    const int nb = r7;
    const int s0 = ROT ? (r7 + 2 * q7 + (xcd >> 1) + 3 * (xcd & 1)) % 7 : 0;
    auto rot = [&](int s) { const int k = s0 + s; return k >= 7 ? k - 7 : k; };
    const unsigned lbase = (unsigned)(size_t)lds;
    const int hh = lane >> 5, blk = (lane >> 4) & 1, q = (lane & 15) >> 2, pp = lane & 3;

    if (__builtin_expect(wave >= 4, 1)) {
        const int w = wave - 4, r = lane >> 3, p = lane & 7;
        const u32x4 rx = make_rsrc(xp, S * D * 2);
        const u32x4 rw = make_rsrc((w & 1) ? wup : wgp, D * F * 2);
        const int vx = r * (D * 2) + ((p ^ (((w & 1) << 2) | (r >> 1))) << 4);
        const int vw = r * (F * 2) + ((((p >> 1) ^ (((r >> 1) & 1) | ((w & 1) << 1))) << 5) | ((p & 1) << 4));
        const int sx = (rb * 128 + 8 * w) * (D * 2);
        const int sw = (8 * (w >> 1)) * (F * 2) + cb * 256;
        const unsigned lw = lbase + w * 1024;
        auto ix = [&](int s) {
            const int so = sx + rot(s) * 128;
#pragma unroll
            for (int i = 0; i < 4; ++i) dma(rx, lw + s * SLOT + i * 4096, vx, so + i * 32 * (D * 2));
        };
        auto iw = [&](int s) {
            const int so = sw + rot(s % 7) * 64 * (F * 2) + (s / 7) * 128;
#pragma unroll
            for (int i = 0; i < 4; ++i) dma(rw, lw + wslot(s) * SLOT + i * 4096, vw, so + i * 16 * (F * 2));
        };
        ix(0); iw(0); ix(1); iw(1);
        STAMP(1);
        const int kh = w >> 1, st = w & 1;
        const u32x4 rd = make_rsrc(wdp, F * D * 2);
        const int vd = r * (D * 2) + ((p ^ (((r >> 1) & 1) << 2)) << 4);
        const int sd = (kh * 7 * 128 + st * 8) * (D * 2) + nb * 128;
        const unsigned ld = lbase + kh * 8192 + st * 1024;
        auto id = [&](int part, int j) {
            const int so = sd + (rot(j) * 128 + part * 64) * (D * 2);
#pragma unroll
            for (int i = 0; i < 4; ++i) dma(rd, ld + dslot(part, j) * SLOT + i * 2048, vd, so + i * 16 * (D * 2));
        };
        WAITV(8);  STAMP(2);  BAR(); STAMP(3);  ix(2); iw(2);
        WAITV(8);  STAMP(4);  BAR(); STAMP(5);  ix(3); iw(3);
        WAITV(8);  STAMP(6);  BAR(); STAMP(7);  ix(4); iw(4);
        WAITV(8);  STAMP(8);  BAR(); STAMP(9);  ix(5); iw(5);
        WAITV(8);  STAMP(10); BAR(); STAMP(11); ix(6); iw(6);
        WAITV(8);  STAMP(12); BAR(); STAMP(13); iw(7);
        WAITV(4);  STAMP(14); BAR(); STAMP(15); iw(8);
        WAITV(4);  STAMP(16); BAR(); STAMP(17); iw(9);
        WAITV(4);  STAMP(18); BAR(); STAMP(19); iw(10); id(0, 0);
        WAITV(8);  STAMP(20); BAR(); STAMP(21); iw(11); id(0, 1);
        WAITV(12); STAMP(22); BAR(); STAMP(23); iw(12); id(0, 2);
        WAITV(12); STAMP(24); BAR(); STAMP(25); iw(13); id(0, 3);
        WAITV(12); STAMP(26); BAR(); STAMP(27); id(0, 4); id(0, 6);
        WAITV(12); STAMP(28); BAR(); STAMP(29); id(0, 5); id(1, 0);
        WAITV(16); STAMP(30); BAR(); STAMP(31);

        const unsigned long long tag = make_tag();
        const u32x4 rh = make_rsrc(ws, H_BYTES);
        {
            gu64* line = flag_line(ws, 0, mb >> 1, mb & 1, kh) + (lane < 14 ? lane : 13);
            unsigned long long v;
            asm volatile("global_load_dwordx2 %0, %1, off sc1" : "=v"(v) : "v"(line) : "memory");
            id(1, 1); id(1, 2);
            asm volatile("s_waitcnt vmcnt(8)" : "+v"(v) :: "memory");
#pragma unroll 1
            for (unsigned spins = 0; spins < (1u << 22); ++spins) {
                if (__all(v == tag)) break;
                asm volatile("global_load_dwordx2 %0, %1, off sc1\n\ts_waitcnt vmcnt(0)" : "=v"(v) : "v"(line) : "memory");
            }
        }
        STAMP(32);
        u32x4 hf[28];
        const int hbase = ((2 * mb + st) * 112 + kh * 56) * 1024;
#pragma unroll
        for (int j = 0; j < 7; ++j) {
            const int so = hbase + rot(j) * 8192;
#pragma unroll
            for (int ks = 0; ks < 4; ++ks) hf[4 * j + ks] = load_sc1(rh, lane * 16, so + ks * 1024);
        }
        STAMP(33);
        unsigned ad[2][3];
#pragma unroll
        for (int nt = 0; nt < 2; ++nt)
#pragma unroll
            for (int k = 0; k < 3; ++k) {
                ad[nt][k] = lbase + k * 65536 + kh * 8192 + (4 * hh + q) * 128 + ((nt ^ (q >> 1)) << 6) + blk * 32 + pp * 8;
                OPAQUE(ad[nt][k]);
            }
        bf16x8 fd[2][2];
        auto rdd = [&](int buf, int nt, int j, int ks) {
            const int o = dslot(0, j) * SLOT + ks * 2048;
            fd[buf][nt] = cat(lds_tr(lp(ad[nt], o)), lds_tr(lp(ad[nt], o + 1024)));
        };
        f32x16 acc2[2] = {};
        rdd(0, 0, 0, 0); rdd(0, 1, 0, 0);
#pragma unroll
        for (int j = 0; j < 7; ++j) {
#pragma unroll
            for (int ks = 0; ks < 4; ++ks) {
                const int cur = ks & 1, nxt = cur ^ 1, n = 4 * j + ks;
                const int nj = ks == 3 ? j + 1 : j, nks = (ks + 1) & 3;
                if (n == 15) {
                    wait_for<12>(hf[15]);
                    STAMP(34); BAR_LGKM(); STAMP(35);
                    id(1, 3); id(1, 4); id(1, 5); id(1, 6);
                }
                if (n < 15) { if (n == 0) wait_for<27>(hf[n]); else if (n == 1) wait_for<26>(hf[n]); else if (n == 2) wait_for<25>(hf[n]); else if (n == 3) wait_for<24>(hf[n]);
                    else if (n == 4) wait_for<23>(hf[n]); else if (n == 5) wait_for<22>(hf[n]); else if (n == 6) wait_for<21>(hf[n]); else if (n == 7) wait_for<20>(hf[n]);
                    else if (n == 8) wait_for<19>(hf[n]); else if (n == 9) wait_for<18>(hf[n]); else if (n == 10) wait_for<17>(hf[n]); else if (n == 11) wait_for<16>(hf[n]);
                    else if (n == 12) wait_for<15>(hf[n]); else if (n == 13) wait_for<14>(hf[n]); else wait_for<13>(hf[n]); }
                if (n > 15) { if (n == 16) wait_for<27>(hf[n]); else if (n == 17) wait_for<26>(hf[n]); else if (n == 18) wait_for<25>(hf[n]); else if (n == 19) wait_for<24>(hf[n]);
                    else if (n == 20) wait_for<23>(hf[n]); else if (n == 21) wait_for<22>(hf[n]); else if (n == 22) wait_for<21>(hf[n]); else if (n == 23) wait_for<20>(hf[n]);
                    else if (n == 24) wait_for<19>(hf[n]); else if (n == 25) wait_for<18>(hf[n]); else if (n == 26) wait_for<17>(hf[n]); else wait_for<16>(hf[n]); }
                const bf16x8 hb = __builtin_bit_cast(bf16x8, hf[n]);
                SCHED();
                acc2[0] = mfma(fd[cur][0], hb, acc2[0]);
                SCHED();
                if (nj < 7) rdd(nxt, 0, nj, nks);
                SCHED();
                acc2[1] = mfma(fd[cur][1], hb, acc2[1]);
                SCHED();
                if (nj < 7) rdd(nxt, 1, nj, nks);
                SCHED();
            }
        }
        STAMP(36);
        WAITV(0); STAMP(37);
        BAR(); STAMP(38);
        {
            const int row = st * 32 + (lane & 31);
            const unsigned pa = lbase + (4 + kh) * SLOT + row * 256 + ((hh ^ (row & 15)) << 4);
#pragma unroll
            for (int nt = 0; nt < 2; ++nt)
#pragma unroll
                for (int g = 0; g < 4; ++g) {
                    f32x4 v = {acc2[nt][4 * g], acc2[nt][4 * g + 1], acc2[nt][4 * g + 2], acc2[nt][4 * g + 3]};
                    *(f32x4 LAS*)(pa ^ (unsigned)((nt * 8 + g * 2) << 4)) = v;
                }
        }
        BAR_LGKM(); STAMP(39);
    } else {
        __builtin_amdgcn_s_setprio(2);
        const __amdgpu_buffer_rsrc_t rh = __builtin_amdgcn_make_buffer_rsrc(ws, 0, H_BYTES, 0x00020000);
        const unsigned long long tag = make_tag();
        {
            const int sw = wave & 1, fw = wave >> 1;
            gu64* mine = flag_line(ws, 0, rb, sw, cb >= 7) + (cb >= 7 ? cb - 7 : cb) * 2 + fw;
            unsigned ax[4][3], aw[2][3];
#pragma unroll
            for (int ks = 0; ks < 4; ++ks)
#pragma unroll
                for (int k = 0; k < 2; ++k) {
                    ax[ks][k] = lbase + k * 65536 + sw * 8192 + (lane & 31) * 128 + (((2 * ks + hh) ^ ((lane >> 1) & 7)) << 4);
                    OPAQUE(ax[ks][k]);
                }
#pragma unroll
            for (int t = 0; t < 2; ++t)
#pragma unroll
                for (int k = 1; k < 3; ++k) {
                    aw[t][k] = lbase + k * 65536 + hh * 2048 + blk * 1024 + q * 128 + pp * 8 + (((2 * fw + t) ^ ((q >> 1) | (blk << 1))) << 5);
                    OPAQUE(aw[t][k]);
                }

            f32x16 acc[2][2][2] = {};
            bf16x8 fa[2][2], fb[2][2];
            auto rd0 = [&](int buf, int s, int ks) {
                const int xo = (s % 7) * SLOT, wo = wslot(s) * SLOT + ks * 4096;
                fa[buf][0] = cat(lds_tr(lp(aw[0], wo)), lds_tr(lp(aw[0], wo + 512)));
                fb[buf][0] = lds_b128(lp(ax[ks], xo));
            };
            auto rd1 = [&](int buf, int s, int ks) {
                const int xo = (s % 7) * SLOT, wo = wslot(s) * SLOT + ks * 4096;
                fb[buf][1] = lds_b128(lp(ax[ks], xo + 4096));
                fa[buf][1] = cat(lds_tr(lp(aw[1], wo)), lds_tr(lp(aw[1], wo + 512)));
            };
            auto hstore = [&](int pass, int st, int t, u32x4 v) {
                const int stile = 4 * rb + 2 * sw + st, kstep = 8 * cb + 4 * pass + 2 * fw + t;
                __builtin_amdgcn_raw_buffer_store_b128(v, rh, lane * 16, (stile * 112 + kstep) * 1024, 16);
            };

            BAR();
            STAMP(2);
            rd0(0, 0, 0); rd1(0, 0, 0);
            u32x4 hv;
#pragma unroll
            for (int s = 0; s < 14; ++s) {
                const int pass = s / 7;
#pragma unroll
                for (int ks = 0; ks < 4; ++ks) {
                    const int cur = ks & 1, nxt = cur ^ 1;
                    const int ns = ks == 3 ? s + 1 : s, nks = (ks + 1) & 3;
                    const bool rdn = ns < 14;
                    const int n = (s - 7) * 4 + ks;
                    const bool ep = s >= 7 && s < 11;
                    const int est = (n >> 3) & 1, et = (n >> 2) & 1, e0 = 2 * (n & 3);
                    float t0, t1, x0, x1, r0, r1;
                    if (ks == 3) {
                        if (s == PUBA) {
                            WAITV(0);
                            if (lane == 0) __hip_atomic_store(mine, tag, __ATOMIC_RELAXED, __HIP_MEMORY_SCOPE_AGENT);
                            STAMP(9);
                        }
                        BAR_LGKM();
                    }
                    SCHED();
                    acc[pass][0][0] = mfma(fa[cur][0], fb[cur][0], acc[pass][0][0]);
                    SCHED();
                    if (rdn) rd0(nxt, ns, nks);
                    if (ep) { t0 = acc[0][est][et][e0] * NLOG2E; OPAQUE(t0); t1 = acc[0][est][et][e0 + 1] * NLOG2E; OPAQUE(t1); }
                    SCHED();
                    acc[pass][1][0] = mfma(fa[cur][0], fb[cur][1], acc[pass][1][0]);
                    SCHED();
                    if (rdn) rd1(nxt, ns, nks);
                    if (ep) { x0 = __builtin_amdgcn_exp2f(t0); OPAQUE(x0); }
                    SCHED();
                    acc[pass][0][1] = mfma(fa[cur][1], fb[cur][0], acc[pass][0][1]);
                    SCHED();
                    if (ep) { x1 = __builtin_amdgcn_exp2f(t1); x0 = 1.0f + x0; OPAQUE(x0); x1 = 1.0f + x1; OPAQUE(x1); r0 = __builtin_amdgcn_rcpf(x0); OPAQUE(r0); }
                    SCHED();
                    acc[pass][1][1] = mfma(fa[cur][1], fb[cur][1], acc[pass][1][1]);
                    SCHED();
                    if (ep) {
                        r1 = __builtin_amdgcn_rcpf(x1);
                        r0 = r0 * acc[0][est][et][e0 + 8]; OPAQUE(r0);
                        r1 = r1 * acc[0][est][et][e0 + 9]; OPAQUE(r1);
                        hv[n & 3] = pack_bf16(r0, r1);
                        if ((n & 3) == 3) hstore(0, est, et, hv);
                        SCHED();
                    }
                }
                if (s == 6) { STAMP(3); }
            }
            STAMP(4);
#pragma unroll
            for (int st = 0; st < 2; ++st)
#pragma unroll
                for (int t = 0; t < 2; ++t) hstore(1, st, t, glu_frag(acc[1][st][t]));
            STAMP(5);
            WAITV(0);
            if (lane == 0) __hip_atomic_store(mine + 16 * 2 * 2 * 16, tag, __ATOMIC_RELAXED, __HIP_MEMORY_SCOPE_AGENT);
            STAMP(6);
        }
        BAR();
        const int kh = wave >> 1, st2 = wave & 1;
        {
            gu64* line = flag_line(ws, 1, mb >> 1, mb & 1, kh) + (lane < 14 ? lane : 13);
#pragma unroll 1
            for (unsigned spins = 0; spins < (1u << 22); ++spins) {
                unsigned long long v = __hip_atomic_load(line, __ATOMIC_RELAXED, __HIP_MEMORY_SCOPE_AGENT);
                if (__all(v == tag)) break;
            }
            asm volatile("" ::: "memory");
        }
        STAMP(7);
        const int hbase = ((2 * mb + st2) * 112 + kh * 56 + 4) * 1024;
        u32x4 hf[28];
#pragma unroll
        for (int j = 0; j < 7; ++j) {
            const int so = hbase + rot(j) * 8192;
#pragma unroll
            for (int ks = 0; ks < 4; ++ks) hf[4 * j + ks] = __builtin_amdgcn_raw_buffer_load_b128(rh, lane * 16, so + ks * 1024, 16);
        }
        SCHED();
        STAMP(8);
        unsigned ad[2][3];
#pragma unroll
        for (int nt = 0; nt < 2; ++nt)
#pragma unroll
            for (int k = 0; k < 3; ++k) {
                ad[nt][k] = lbase + k * 65536 + kh * 8192 + (4 * hh + q) * 128 + ((nt ^ (q >> 1)) << 6) + blk * 32 + pp * 8;
                OPAQUE(ad[nt][k]);
            }
        bf16x8 fd[2][2];
        auto rdd = [&](int buf, int nt, int j, int ks) {
            const int o = dslot(1, j) * SLOT + ks * 2048;
            fd[buf][nt] = cat(lds_tr(lp(ad[nt], o)), lds_tr(lp(ad[nt], o + 1024)));
        };
        f32x16 acc2[2] = {};
        BAR();
        STAMP(10);
        rdd(0, 0, 0, 0); rdd(0, 1, 0, 0);
#pragma unroll
        for (int j = 0; j < 7; ++j) {
#pragma unroll
            for (int ks = 0; ks < 4; ++ks) {
                const int cur = ks & 1, nxt = cur ^ 1, n = 4 * j + ks;
                const int nj = ks == 3 ? j + 1 : j, nks = (ks + 1) & 3;
                if (n == 7) BAR_LGKM();
                const bf16x8 hb = __builtin_bit_cast(bf16x8, hf[n]);
                SCHED();
                acc2[0] = mfma(fd[cur][0], hb, acc2[0]);
                SCHED();
                if (nj < 7) rdd(nxt, 0, nj, nks);
                SCHED();
                acc2[1] = mfma(fd[cur][1], hb, acc2[1]);
                SCHED();
                if (nj < 7) rdd(nxt, 1, nj, nks);
                SCHED();
            }
        }
        STAMP(11);
        {
            const int row = st2 * 32 + (lane & 31);
            const unsigned pc = lbase + (7 + kh) * SLOT + row * 256 + ((hh ^ (row & 15)) << 4);
#pragma unroll
            for (int nt = 0; nt < 2; ++nt)
#pragma unroll
                for (int g = 0; g < 4; ++g) {
                    f32x4 v = {acc2[nt][4 * g], acc2[nt][4 * g + 1], acc2[nt][4 * g + 2], acc2[nt][4 * g + 3]};
                    *(f32x4 LAS*)(pc ^ (unsigned)((nt * 8 + g * 2) << 4)) = v;
                }
        }
        asm volatile("7702:");
    }
    BAR_LGKM();
    {
        const int row = tid >> 3, seg = tid & 7;
        const unsigned a0 = lbase + row * 256 + (((2 * seg) ^ (row & 15)) << 4), a1 = a0 ^ 16u;
        f32x4 lo = *(const f32x4 LAS*)(a0 + 4 * SLOT), hi = *(const f32x4 LAS*)(a1 + 4 * SLOT);
        lo += *(const f32x4 LAS*)(a0 + 5 * SLOT); hi += *(const f32x4 LAS*)(a1 + 5 * SLOT);
        lo += *(const f32x4 LAS*)(a0 + 7 * SLOT); hi += *(const f32x4 LAS*)(a1 + 7 * SLOT);
        lo += *(const f32x4 LAS*)(a0 + 8 * SLOT); hi += *(const f32x4 LAS*)(a1 + 8 * SLOT);
        u32x4 o;
        o.x = pack_bf16(lo[0], lo[1]); o.y = pack_bf16(lo[2], lo[3]);
        o.z = pack_bf16(hi[0], hi[1]); o.w = pack_bf16(hi[2], hi[3]);
        const __amdgpu_buffer_rsrc_t ro = __builtin_amdgcn_make_buffer_rsrc(outp, 0, S * D * 2, 0x00020000);
        __builtin_amdgcn_raw_buffer_store_b128(o, ro, row * (D * 2) + seg * 16, mb * 64 * (D * 2) + nb * 128, 16);
    }
    STAMP(61); STAMPR(63); STAMP_OUT();
}

}

extern "C" void kernel_launch(void* const* d_in, const int* in_sizes, int n_in, void* d_out, int out_size, void* d_ws,
                              size_t ws_size, hipStream_t stream) {
    (void)in_sizes; (void)n_in; (void)out_size; (void)ws_size;
    glu_fused<<<GRID, 512, 0, stream>>>(d_in[0], d_in[1], d_in[2], d_in[3], d_out, d_ws);
}
```

```cpp
#include <hip/hip_runtime.h>
#include <stdint.h>
#include <utility>

typedef short bf16x8 __attribute__((ext_vector_type(8)));
typedef short s16x4 __attribute__((ext_vector_type(4)));
typedef float f32x16 __attribute__((ext_vector_type(16)));
typedef float f32x4 __attribute__((ext_vector_type(4)));
typedef float f32x2 __attribute__((ext_vector_type(2)));
typedef __bf16 bf16x2 __attribute__((ext_vector_type(2)));
typedef unsigned u32x4 __attribute__((ext_vector_type(4)));
typedef unsigned u32x8 __attribute__((ext_vector_type(8)));
#define LAS __attribute__((address_space(3)))
#define GAS __attribute__((address_space(1)))

extern "C" __device__ unsigned long long glu_dispatch_id() __asm("llvm.amdgcn.dispatch.id");

namespace {

constexpr int S = 2048, D = 448, F = 1792;
constexpr int SLOT = 16384;
constexpr int NWG = 224;
constexpr int NHELP = 32;
constexpr unsigned H_BYTES = (unsigned)S * F * 2;
constexpr unsigned NPROD = 2 * 16 * 2 * 2 * 14;
constexpr unsigned FIN_OFF = H_BYTES;
constexpr unsigned WS_USED = FIN_OFF + NPROD * 128;
constexpr int PD = 8;
constexpr int NPRO = 4;
constexpr int HB0 = 12;
constexpr int BPOLL = 3;

__device__ constexpr int wslot(int s) { return 7 + s % 3; }
__device__ constexpr int dslot(int part, int j) { return part == 0 ? (j < 6 ? j : 9) : (j == 0 ? 7 : j - 1); }

#define WAITV(n) asm volatile("s_waitcnt vmcnt(" #n ")" ::: "memory")
#define BAR() asm volatile("s_barrier" ::: "memory")
#define BAR_LGKM() asm volatile("s_waitcnt lgkmcnt(0)\n\ts_barrier" ::: "memory")
#define SCHED() __builtin_amdgcn_sched_barrier(0)
#define OPAQUE(v) asm volatile("" : "+v"(v))

__device__ __forceinline__ const char LAS* lp(const unsigned (&base)[3], int off) {
    return (const char LAS*)(base[off >> 16] + (unsigned)(off & 0xFFFF));
}
__device__ __forceinline__ bf16x8 lds_b128(const char LAS* p) { return *(const bf16x8 LAS*)p; }
__device__ __forceinline__ s16x4 lds_tr(const char LAS* p) { return __builtin_amdgcn_ds_read_tr16_b64_v4i16((s16x4 LAS*)p); }
__device__ __forceinline__ bf16x8 cat(s16x4 a, s16x4 b) { return __builtin_shufflevector(a, b, 0, 1, 2, 3, 4, 5, 6, 7); }
__device__ __forceinline__ f32x16 mfma(bf16x8 a, bf16x8 b, f32x16 c) {
    return __builtin_amdgcn_mfma_f32_32x32x16_bf16(a, b, c, 0, 0, 0);
}
__device__ __forceinline__ unsigned pack_bf16(float a, float b) {
    f32x2 v = {a, b};
    bf16x2 r = __builtin_convertvector(v, bf16x2);
    return __builtin_bit_cast(unsigned, r);
}
constexpr float NLOG2E = -1.4426950408889634f;
__device__ __forceinline__ u32x4 make_rsrc(const void* p, unsigned bytes) {
    const unsigned long long a = (unsigned long long)p;
    u32x4 r = {(unsigned)a, (unsigned)(a >> 32) & 0xFFFFu, bytes, 0x00020000u};
    return r;
}
__device__ __forceinline__ void dma(const u32x4& rs, unsigned ldsaddr, int voff, int soff) {
    asm volatile("s_mov_b32 m0, %0\n\ts_nop 0\n\tbuffer_load_dwordx4 %1, %2, %3 offen lds"
                 :: "s"(ldsaddr), "v"(voff), "s"(rs), "s"(soff) : "memory");
}
__device__ __forceinline__ unsigned long long make_tag() {
    return (glu_dispatch_id() + 1ull) * 0x9E3779B97F4A7C15ull ^ ((unsigned long long)__builtin_amdgcn_dispatch_ptr() * 0xD1B54A32D192ED03ull);
}
__device__ __forceinline__ int prod_index(int pass, int rb, int half, int fw, int cb) { return (((pass * 16 + rb) * 2 + half) * 2 + fw) * 14 + cb; }
__device__ __forceinline__ unsigned vm_pending() {
    unsigned v;
    asm volatile("s_getreg_b32 %0, hwreg(HW_REG_IB_STS)" : "=s"(v));
    return (v & 15u) | ((v >> 18) & 0x30u);
}
template <int... I, class Fn> __device__ __forceinline__ void static_for_impl(std::integer_sequence<int, I...>, Fn&& f) {
    (f(std::integral_constant<int, I>{}), ...);
}
template <int N, class Fn> __device__ __forceinline__ void static_for(Fn&& f) { static_for_impl(std::make_integer_sequence<int, N>{}, f); }
template <int N> __device__ __forceinline__ void waitv() { asm volatile("s_waitcnt vmcnt(%0)" :: "n"(N) : "memory"); }

__device__ __forceinline__ void chunk_load(u32x4& a, u32x4& b, u32x4& d, u32x4& e, int l16, const u32x4& rs, int s0, int s1) {
    asm volatile("buffer_load_dwordx4 %0, %4, %5, %6 offen sc1\n\t"
                 "buffer_load_dwordx4 %1, %4, %5, %7 offen sc1\n\t"
                 "buffer_load_dwordx4 %2, %4, %5, %6 offen offset:1024 sc1\n\t"
                 "buffer_load_dwordx4 %3, %4, %5, %7 offen offset:1024 sc1"
                 : "=&v"(a), "=&v"(b), "=&v"(d), "=&v"(e)
                 : "v"(l16), "s"(rs), "s"(s0), "s"(s1) : "memory");
}
template <int K> __device__ __forceinline__ void chunk_wait(u32x4& a, u32x4& b, u32x4& d, u32x4& e) {
    asm volatile("s_waitcnt vmcnt(%4)" : "+v"(a), "+v"(b), "+v"(d), "+v"(e) : "n"(K) : "memory");
}
__device__ __forceinline__ void keep2(unsigned long long& a, unsigned long long& b) { asm volatile("" : "+v"(a), "+v"(b)); }
__device__ __forceinline__ void flag_poll(unsigned long long& v, int vf, const u32x4& rs, int so) {
    asm volatile("buffer_load_dwordx2 %0, %1, %2, %3 offen sc1" : "=v"(v) : "v"(vf), "s"(rs), "s"(so) : "memory");
}
template <int K, int SL> __device__ __forceinline__ void flag_wait(unsigned long long& v, unsigned long long tag, int vf, const u32x4& rs, int so) {
    unsigned cnt;
    asm volatile("s_waitcnt vmcnt(%[k])\n\t"
                 "v_cmp_ne_u64 vcc, %[tag], %[a]\n\t"
                 "s_cbranch_vccz 2f\n\t"
                 "s_mov_b32 %[cnt], 0x200000\n"
                 "1:\n\t"
                 "buffer_load_dwordx2 %[a], %[vf], %[rs], %[so] offen sc1\n\t"
                 "s_waitcnt vmcnt(0)\n\t"
                 "v_cmp_ne_u64 vcc, %[tag], %[a]\n\t"
                 "s_cbranch_vccz 2f\n\t"
                 "s_sleep %[sl]\n\t"
                 "s_sub_u32 %[cnt], %[cnt], 1\n\t"
                 "s_cbranch_scc0 1b\n"
                 "2:"
                 : [a] "+v"(v), [cnt] "=&s"(cnt)
                 : [k] "n"(K), [tag] "s"(tag), [vf] "v"(vf), [rs] "s"(rs), [so] "s"(so), [sl] "n"(SL) : "vcc", "scc", "memory");
}
template <int K0, int K1> __device__ __forceinline__ void flag_wait2(unsigned long long& v0, unsigned long long& v1, unsigned long long tag, int vf, const u32x4& rs, int so) {
    unsigned cnt;
    asm volatile("s_waitcnt vmcnt(%[k0])\n\t"
                 "v_cmp_ne_u64 vcc, %[tag], %[a]\n\t"
                 "s_cbranch_vccz 2f\n\t"
                 "s_waitcnt vmcnt(%[k1])\n\t"
                 "v_cmp_ne_u64 vcc, %[tag], %[b]\n\t"
                 "s_cbranch_vccz 2f\n\t"
                 "s_mov_b32 %[cnt], 0x200000\n"
                 "1:\n\t"
                 "buffer_load_dwordx2 %[a], %[vf], %[rs], %[so] offen sc1\n\t"
                 "s_waitcnt vmcnt(0)\n\t"
                 "v_cmp_ne_u64 vcc, %[tag], %[a]\n\t"
                 "s_cbranch_vccz 2f\n\t"
                 "s_sub_u32 %[cnt], %[cnt], 1\n\t"
                 "s_cbranch_scc0 1b\n"
                 "2:"
                 : [a] "+v"(v0), [b] "+v"(v1), [cnt] "=&s"(cnt)
                 : [k0] "n"(K0), [k1] "n"(K1), [tag] "s"(tag), [vf] "v"(vf), [rs] "s"(rs), [so] "s"(so) : "vcc", "scc", "memory");
}

constexpr int imax(int a, int b) { return a > b ? a : b; }
constexpr int hiss(int c) {
    return c < NPRO ? -1 : c < 7 ? imax(c - NPRO, 2 * c - PD) : imax(HB0 + (c - 7), 2 * (c - 7) + 2);
}
constexpr int NDM = 32;
constexpr int dpart(int n) { return n < 4 ? 0 : 1; }
constexpr int dchunk(int n) { return n < 4 ? 5 : (n - 4) / 4; }
constexpr int dmin(int n) { return n < 8 ? 0 : 2 * (dchunk(n) - 1) + ((n & 3) >= 2); }
constexpr int diss(int n) { const int g = n * 3 / 8; return g > dmin(n) ? g : dmin(n); }
constexpr int NB = hiss(7);
constexpr int PB = NB - BPOLL;
constexpr int ops_at(int pos) {
    int n = 0;
    for (int c = 0; c < 14; ++c) if (8 * hiss(c) + 3 == pos) n += 4;
    for (int d = 0; d < NDM; ++d) if (8 * diss(d) + 4 == pos) n += 1;
    if (8 * PB + 5 == pos) n += 1;
    return n;
}
constexpr int ops_between(int from, int to) { int n = 0; for (int p = from + 1; p < to; ++p) n += ops_at(p); return n; }
constexpr int younger_chunk(int c) {
    int n = ops_between(8 * hiss(c) + 3, 16 * c);
    for (int k = c + 1; k < 14; ++k) if (hiss(k) == hiss(c)) n += 4;
    return n;
}
constexpr int younger_dma(int d, int g) {
    int n = ops_between(8 * diss(d) + 4, 8 * g + 1);
    for (int k = d + 1; k < NDM; ++k) if (diss(k) == diss(d)) n += 1;
    return n;
}
constexpr int dneed(int g) {
    const int h = g + 1, part = h / 14, j = (h % 14) >> 1, t = h & 1;
    if (h >= 28) return -1;
    const int n = part == 0 ? (j == 5 ? 0 : -4) : 4 + 4 * j;
    return n < 0 ? -1 : n + 2 * t + 1;
}
constexpr int younger_poll() { return ops_between(8 * PB + 5, 8 * NB + 2); }
constexpr bool sched_ok() {
    for (int c = 7; c < 14; ++c) if (hiss(c) < 2 * (c - 7) + 2) return false;
    for (int c = 0; c < 14; ++c) if (hiss(c) >= 2 * c || younger_chunk(c) > 63) return false;
    for (int g = 0; g < 27; ++g) if (dneed(g) >= 0 && (diss(dneed(g)) >= g || younger_dma(dneed(g), g) > 63)) return false;
    return PB >= 0 && NPRO <= 7;
}
typedef char schedule_is_consistent[sched_ok() ? 1 : -1];

#define KPTR(k, i) ((char*)(((unsigned long long)(k)[(i) + 1] << 32) | (k)[i]))
#define ARGS() asm volatile("s_waitcnt lgkmcnt(0)" : "+s"(ka), "+s"(kb))

__global__ __launch_bounds__(512, 2) void glu_fused(const void* a0, const void* a1, const void* a2, const void* a3, void* a4, void* a5) {
    __shared__ __attribute__((aligned(1024))) char lds_raw[10 * SLOT];
    u32x8 ka; u32x4 kb;
    asm volatile(".Lglu_b:\n\ts_load_dwordx8 %0, %2, 0x0\n\ts_load_dwordx4 %1, %2, 0x20"
                 : "=&s"(ka), "=&s"(kb) : "s"(__builtin_amdgcn_kernarg_segment_ptr()));
    const int tid = threadIdx.x, lane = tid & 63;
    const int wave = __builtin_amdgcn_readfirstlane(tid >> 6);
    const int xcd = blockIdx.x & 7;
    const int sl = (int)(blockIdx.x >> 3) - NHELP / 8;
    const int q7 = (sl * 37) >> 8, r7 = sl - 7 * q7;
    const unsigned lbase = (unsigned)(size_t)(char LAS*)lds_raw;
    const int rb = 4 * (xcd >> 1) + q7;
    const int cb = 7 * (xcd & 1) + r7;
    const int mb = 4 * xcd + q7;
    const int nb = r7;
    const int hh = lane >> 5, blk = (lane >> 4) & 1, q = (lane & 15) >> 2, pp = lane & 3;
    const int w = wave & 3, sw = w & 1, fw = w >> 1, r = lane >> 3, p = lane & 7;
    int vx = r * (D * 2) + ((p ^ (((w & 1) << 2) | (r >> 1))) << 4);
    int vw = r * (F * 2) + ((((p >> 1) ^ (((r >> 1) & 1) | ((w & 1) << 1))) << 5) | ((p & 1) << 4));
    int sx = (rb * 128 + 8 * w) * (D * 2);
    int sw_ = (8 * (w >> 1)) * (F * 2) + cb * 256;
    unsigned lw = lbase + w * 1024;
    OPAQUE(vx); OPAQUE(vw);
    asm volatile("" : "+s"(sx), "+s"(sw_), "+s"(lw));

    ARGS();

    if (__builtin_expect(sl < 0, 0)) {
        const char* const xp = KPTR(ka, 0); const char* const wgp = KPTR(ka, 2); const char* const wup = KPTR(ka, 4);
        const char* const wdp = KPTR(ka, 6);
        const int hid = sl + NHELP / 8;
        const int c14 = lane % 14, l7 = lane % 7;
        const char* px = xp + (size_t)((xcd >> 1) * 512 + (xcd & 1) * 256 + lane) * (D * 2);
        const char* pw = (c14 >= 7 ? wup : wgp) + (size_t)((xcd >> 1) * 16 + lane / 14) * (F * 2) + ((xcd & 1) * 7 + (c14 >= 7 ? c14 - 7 : c14)) * 256;
        const char* pd = wdp + (size_t)(lane / 7) * (D * 2) + l7 * 128;
        const unsigned ldst = lbase + wave * 256;
        auto touch = [&](const char* p_) { asm volatile("s_mov_b32 m0, %0\n\ts_nop 0\n\tglobal_load_lds_dword %1, off" :: "s"(ldst), "v"(p_) : "memory"); };
#pragma unroll
        for (int it = 0; it < 4; ++it) {
            const int n = it * 32 + wave * 4 + hid;
            if (n < 56) {
                const int s = n >> 3, m = n & 7;
                if (m < 4) touch(px + (size_t)(m * 64) * (D * 2) + s * 128);
                else if (lane < 56) touch(pw + (size_t)(s * 64 + (m - 4) * 4) * (F * 2));
            } else if (n < 84) {
                const int s = (n - 56) >> 2, m = (n - 56) & 3;
                if (lane < 56) touch(pw + (size_t)(s * 64 + m * 4) * (F * 2) + 128);
            } else if (n < 112) {
                const int e = (n - 84) >> 1, part = e >= 7, j = e - 7 * part, R = xcd * 16 + (n & 1) * 8;
                if (lane < 56) touch(pd + (size_t)((j + 7 * (R >> 6)) * 128 + part * 64 + (R & 63)) * (D * 2));
            }
            if (it == 0 && wave == 7) {
                unsigned long long pc; unsigned back, len;
                asm volatile("s_getpc_b64 %0\n.Lglu_p:\n\ts_mov_b32 %1, .Lglu_p-.Lglu_b\n\ts_mov_b32 %2, .Lglu_e1-.Lglu_b\n\t"
                             "s_max_u32 %2, %2, .Lglu_e2-.Lglu_b\n\ts_max_u32 %2, %2, .Lglu_e3-.Lglu_b" : "=s"(pc), "=s"(back), "=s"(len) :: "scc");
                const unsigned off = (unsigned)(hid * 64 + lane) * 128u;
                if (off < len) touch((const char*)(pc - back) + off);
            }
        }
        WAITV(0);
        return;
    }

    unsigned ax[4][2], aw[2][3];
    auto mm_addr = [&]() {
#pragma unroll
        for (int ks = 0; ks < 4; ++ks)
#pragma unroll
            for (int k = 0; k < 2; ++k) {
                ax[ks][k] = lbase + k * 65536 + sw * 8192 + (lane & 31) * 128 + (((2 * ks + hh) ^ ((lane >> 1) & 7)) << 4);
                OPAQUE(ax[ks][k]);
            }
#pragma unroll
        for (int t = 0; t < 2; ++t)
#pragma unroll
            for (int k = 1; k < 3; ++k) {
                aw[t][k] = lbase + k * 65536 + hh * 2048 + blk * 1024 + q * 128 + pp * 8 + (((2 * fw + t) ^ ((q >> 1) | (blk << 1))) << 5);
                OPAQUE(aw[t][k]);
            }
    };
    f32x16 acc[2][2] = {};
    bf16x8 fa[2][2], fb[2][2];
    auto rd0 = [&](int buf, int s, int ks) {
        const char LAS* xb = (const char LAS*)(ax[ks][((s % 7) * SLOT) >> 16] + (unsigned)(((s % 7) * SLOT) & 0xFFFF));
        const int wo = wslot(s) * SLOT + ks * 4096;
        fa[buf][0] = cat(lds_tr(lp(aw[0], wo)), lds_tr(lp(aw[0], wo + 512)));
        fb[buf][0] = lds_b128(xb);
    };
    auto rd1 = [&](int buf, int s, int ks) {
        const char LAS* xb = (const char LAS*)(ax[ks][((s % 7) * SLOT) >> 16] + (unsigned)(((s % 7) * SLOT) & 0xFFFF));
        const int wo = wslot(s) * SLOT + ks * 4096;
        fb[buf][1] = lds_b128(xb + 4096);
        fa[buf][1] = cat(lds_tr(lp(aw[1], wo)), lds_tr(lp(aw[1], wo + 512)));
    };
    u32x4 hv;
    auto hstore = [&](const __amdgpu_buffer_rsrc_t& rhs, int pass, int f) {
        const int stile = 4 * rb + 2 * sw + (f >> 1), kstep = 8 * cb + 4 * pass + 2 * fw + (f & 1);
        __builtin_amdgcn_raw_buffer_store_b128(hv, rhs, lane * 16, (stile * 112 + kstep) * 1024, 16);
        SCHED();
    };
    auto set_flag = [&](const __amdgpu_buffer_rsrc_t& rhs, unsigned off, unsigned long long tag) {
        const u32x4 v = {(unsigned)tag, (unsigned)(tag >> 32), (unsigned)tag, (unsigned)(tag >> 32)};
        if (lane < 8) __builtin_amdgcn_raw_buffer_store_b128(v, rhs, lane * 16, off, 16);
    };
    auto pair = [&](const __amdgpu_buffer_rsrc_t& rhs, int pass, int p) __attribute__((always_inline)) {
        const int st = p >> 2, t = (p >> 1) & 1, e = 4 * (p & 1);
        float z[4];
#pragma unroll
        for (int i = 0; i < 4; ++i) z[i] = acc[st][t][e + i] * NLOG2E;
        SCHED();
#pragma unroll
        for (int i = 0; i < 4; ++i) z[i] = __builtin_amdgcn_exp2f(z[i]);
        SCHED();
#pragma unroll
        for (int i = 0; i < 4; ++i) z[i] = 1.0f + z[i];
        SCHED();
#pragma unroll
        for (int i = 0; i < 4; ++i) z[i] = __builtin_amdgcn_rcpf(z[i]);
        SCHED();
#pragma unroll
        for (int i = 0; i < 4; ++i) { z[i] = acc[st][t][e + 8 + i] * z[i]; OPAQUE(z[i]); }
        SCHED();
        hv[2 * (p & 1)] = pack_bf16(z[0], z[1]); hv[2 * (p & 1) + 1] = pack_bf16(z[2], z[3]);
        SCHED();
        if (p & 1) hstore(rhs, pass, p >> 1);
    };
    auto quad = [&](const __amdgpu_buffer_rsrc_t& rhs, int pass, int f, const f32x2& c2, const f32x2& one2) {
        const f32x16& a = acc[f >> 1][f & 1];
        f32x2 z[4];
#pragma unroll
        for (int i = 0; i < 4; ++i) z[i] = (f32x2){a[2 * i], a[2 * i + 1]} * c2;
        SCHED();
#pragma unroll
        for (int i = 0; i < 4; ++i) { z[i][0] = __builtin_amdgcn_exp2f(z[i][0]); z[i][1] = __builtin_amdgcn_exp2f(z[i][1]); }
        SCHED();
#pragma unroll
        for (int i = 0; i < 4; ++i) z[i] = z[i] + one2;
        SCHED();
#pragma unroll
        for (int i = 0; i < 4; ++i) { z[i][0] = __builtin_amdgcn_rcpf(z[i][0]); z[i][1] = __builtin_amdgcn_rcpf(z[i][1]); }
        SCHED();
#pragma unroll
        for (int i = 0; i < 4; ++i) z[i] = (f32x2){a[8 + 2 * i], a[9 + 2 * i]} * z[i];
        SCHED();
#pragma unroll
        for (int i = 0; i < 4; ++i) hv[i] = pack_bf16(z[i][0], z[i][1]);
        SCHED();
        hstore(rhs, pass, f);
    };

    if (__builtin_expect(wave < 4, 1)) {
        const u32x4 rx = make_rsrc(KPTR(ka, 0), S * D * 2);
        const u32x4 rw = make_rsrc((w & 1) ? KPTR(ka, 4) : KPTR(ka, 2), D * F * 2);
        auto ix = [&](int s) {
            const int so = sx + s * 128;
#pragma unroll
            for (int i = 0; i < 4; ++i) dma(rx, lw + s * SLOT + i * 4096, vx, so + i * 32 * (D * 2));
        };
        auto iw1 = [&](int s, int i) {
            dma(rw, lw + wslot(s) * SLOT + i * 4096, vw, sw_ + (s % 7) * 64 * (F * 2) + (s / 7) * 128 + i * 16 * (F * 2));
        };
        auto iw = [&](int s) {
#pragma unroll
            for (int i = 0; i < 4; ++i) iw1(s, i);
        };
        ix(0); iw(0);
        ix(1); iw(1);
        SCHED();
        char* const ws = KPTR(kb, 2);
        const __amdgpu_buffer_rsrc_t rhs = __builtin_amdgcn_make_buffer_rsrc(ws, 0, WS_USED, 0x00020000);
        const unsigned long long tag = make_tag();
        mm_addr();
        WAITV(8); BAR(); ix(2); iw(2);
        WAITV(8); BAR(); ix(3); iw(3);
        WAITV(8); BAR(); ix(4); iw(4);
        WAITV(8); BAR(); ix(5); iw(5);
        WAITV(8); BAR(); ix(6); iw(6);
        WAITV(8); BAR(); iw(7);
        WAITV(4); BAR(); iw(8);
        WAITV(4); BAR();
        __builtin_amdgcn_s_setprio(2);
        rd0(0, 7, 0); rd1(0, 7, 0);
#pragma unroll
        for (int s = 7; s < 14; ++s) {
#pragma unroll
            for (int ks = 0; ks < 4; ++ks) {
                const int cur = ks & 1, nxt = cur ^ 1;
                const int ns = ks == 3 ? s + 1 : s, nks = (ks + 1) & 3;
                const bool rdn = ns < 14;
                if (ks == 3 && s < 13) {
                    if (s < 12) { iw1(s + 2, 3); WAITV(4); } else { WAITV(0); }
                    BAR_LGKM();
                }
                SCHED();
                acc[0][0] = mfma(fa[cur][0], fb[cur][0], acc[0][0]);
                SCHED();
                if (rdn) rd0(nxt, ns, nks);
                SCHED();
                acc[1][0] = mfma(fa[cur][0], fb[cur][1], acc[1][0]);
                SCHED();
                if (rdn) rd1(nxt, ns, nks);
                SCHED();
                acc[0][1] = mfma(fa[cur][1], fb[cur][0], acc[0][1]);
                SCHED();
                if (ks < 3 && s < 12) iw1(s + 2, ks);
                SCHED();
                acc[1][1] = mfma(fa[cur][1], fb[cur][1], acc[1][1]);
                SCHED();
            }
        }
        __builtin_amdgcn_s_setprio(3);
        {
            f32x2 c2 = {NLOG2E, NLOG2E}, one2 = {1.0f, 1.0f};
            asm volatile("" : "+s"(c2), "+s"(one2));
#pragma unroll
            for (int f = 0; f < 4; ++f) quad(rhs, 1, f, c2, one2);
        }
        WAITV(0);
        set_flag(rhs, FIN_OFF + prod_index(1, rb, sw, fw, cb) * 128, tag);
        asm volatile(".Lglu_e1:");
    } else {
        const char* const wdp = KPTR(ka, 6); char* const ws = KPTR(kb, 2);
        const __amdgpu_buffer_rsrc_t rhs = __builtin_amdgcn_make_buffer_rsrc(ws, 0, WS_USED, 0x00020000);
        __builtin_amdgcn_s_setprio(2);
        const unsigned long long tag = make_tag();
        mm_addr();
        const u32x4 rd = make_rsrc(wdp, F * D * 2);
        const u32x4 rh = make_rsrc(ws, WS_USED);
        const int vd = r * (D * 2) + ((p ^ (((r >> 1) & 1) << 2)) << 4);
        const int sd = ((w >> 1) * 7 * 128 + (w & 1) * 32) * (D * 2) + nb * 128;
        const unsigned ld = lbase + w * 4096;
        auto id1 = [&](int part, int j, int i) {
            dma(rd, ld + dslot(part, j) * SLOT + i * 1024, vd, sd + (j * 128 + part * 64 + i * 8) * (D * 2));
        };
        auto id = [&](int part, int j) {
#pragma unroll
            for (int i = 0; i < 4; ++i) id1(part, j, i);
        };
        BAR();

        rd0(0, 0, 0); rd1(0, 0, 0);
#pragma unroll
        for (int s = 0; s < 7; ++s) {
#pragma unroll
            for (int ks = 0; ks < 4; ++ks) {
                const int cur = ks & 1, nxt = cur ^ 1;
                const int ns = ks == 3 ? s + 1 : s, nks = (ks + 1) & 3;
                const bool rdn = ns < 7;
                if (ks == 3) BAR_LGKM();
                SCHED();
                acc[0][0] = mfma(fa[cur][0], fb[cur][0], acc[0][0]);
                SCHED();
                if (rdn) rd0(nxt, ns, nks);
                SCHED();
                acc[1][0] = mfma(fa[cur][0], fb[cur][1], acc[1][0]);
                SCHED();
                if (rdn) rd1(nxt, ns, nks);
                SCHED();
                acc[0][1] = mfma(fa[cur][1], fb[cur][0], acc[0][1]);
                SCHED();
                acc[1][1] = mfma(fa[cur][1], fb[cur][1], acc[1][1]);
                SCHED();
            }
        }
        __builtin_amdgcn_s_setprio(1);
        const unsigned mine = FIN_OFF + prod_index(0, rb, sw, fw, cb) * 128;
        bool pub = false;
        auto publish = [&]() { set_flag(rhs, mine, tag); pub = true; };
#define TRYPUB(K) do { if (!pub && vm_pending() <= (K)) { waitv<(K)>(); publish(); } } while (0)
#define IC(v) std::integral_constant<int, (v)>{}
        auto work = [&](auto p0c) __attribute__((always_inline)) {
            static_for<4>([&](auto kc) __attribute__((always_inline)) {
                pair(rhs, 0, decltype(p0c)::value + decltype(kc)::value);
                SCHED();
            });
        };
        auto idp = [&](auto jc, auto kc) {
            constexpr int j = decltype(jc)::value, K = decltype(kc)::value;
            static_for<4>([&](auto ic) {
                constexpr int i = decltype(ic)::value;
                id1(0, j, i);
                TRYPUB(K + i + 1);
            });
        };
        work(IC(0));
        BAR(); SCHED();
        work(IC(4));
        BAR(); SCHED();
        idp(IC(0), IC(0)); idp(IC(1), IC(4));
        BAR(); SCHED();
        TRYPUB(8); idp(IC(2), IC(8));
        BAR();
        TRYPUB(12); idp(IC(3), IC(12));
        BAR();
        const int vf = (lane < 7 ? lane : 6) * 128;
        const int pa = prod_index(0, mb >> 1, mb & 1, w & 1, 7 * (w >> 1));
        unsigned long long v0, v1;
        auto poll = [&](unsigned long long& v, int so) { flag_poll(v, vf, rh, so); };
        poll(v0, FIN_OFF + pa * 128);
        id(0, 4); id(0, 6); TRYPUB(16 + 9);
        BAR();
        poll(v1, FIN_OFF + pa * 128);
        __builtin_amdgcn_s_setprio(1);
        flag_wait2<9, 0>(v0, v1, tag, vf, rh, FIN_OFF + pa * 128);
        if (!pub) publish();

        u32x4 hf[28];
        const int hbase = (2 * mb * 112 + 56 * (w >> 1) + 2 * (w & 1)) * 1024;
        const int l16 = lane * 16;
        unsigned ad[2][3];
#pragma unroll
        for (int nt = 0; nt < 2; ++nt)
#pragma unroll
            for (int k = 0; k < 3; ++k) {
                ad[nt][k] = lbase + k * 65536 + w * 4096 + (4 * hh + q) * 128 + ((nt ^ (q >> 1)) << 6) + blk * 32 + pp * 8;
                OPAQUE(ad[nt][k]);
            }
        bf16x8 fd[2][2];
        f32x16 acc2[2][2] = {};
        auto cload = [&](int c) {
            const int part = c / 7, j = c % 7;
            const int so = hbase + (j * 8 + part * 4) * 1024;
            chunk_load(hf[4 * j], hf[4 * j + 1], hf[4 * j + 2], hf[4 * j + 3], l16, rh, so, so + 112 * 1024);
        };
        auto cwait = [&](auto cc) {
            constexpr int c = decltype(cc)::value, j = c % 7;
            chunk_wait<younger_chunk(c)>(hf[4 * j], hf[4 * j + 1], hf[4 * j + 2], hf[4 * j + 3]);
        };
        auto rdd = [&](int buf, int nt, int g) {
            const int o = dslot(g / 14, (g % 14) >> 1) * SLOT + (g & 1) * 2048;
            fd[buf][nt] = cat(lds_tr(lp(ad[nt], o)), lds_tr(lp(ad[nt], o + 1024)));
        };
        static_for<14>([&](auto cc) { if (hiss(decltype(cc)::value) < 0) cload(decltype(cc)::value); });
        rdd(0, 0, 0); rdd(0, 1, 0);
        unsigned long long vb;
        static_for<28>([&](auto gc) {
            constexpr int g = decltype(gc)::value, n = g % 14, j = n >> 1, t = n & 1;
            constexpr int cur = g & 1, nxt = cur ^ 1;
            if (t == 0) cwait(IC(g >> 1));
            if (g == 0) keep2(v0, v1);
            const bf16x8 h0 = __builtin_bit_cast(bf16x8, hf[4 * j + 2 * t]), h1 = __builtin_bit_cast(bf16x8, hf[4 * j + 2 * t + 1]);
            SCHED();
            acc2[0][0] = mfma(fd[cur][0], h0, acc2[0][0]);
            SCHED();
            if constexpr (dneed(g) >= 0) waitv<younger_dma(dneed(g) < 0 ? 0 : dneed(g), g)>();
            if (g < 27) rdd(nxt, 0, g + 1);
            SCHED();
            acc2[1][0] = mfma(fd[cur][0], h1, acc2[1][0]);
            SCHED();
            if (g < 27) rdd(nxt, 1, g + 1);
            SCHED();
            acc2[0][1] = mfma(fd[cur][1], h0, acc2[0][1]);
            SCHED();
            if (g == NB) {
                flag_wait<younger_poll(), 2>(vb, tag, vf, rh, FIN_OFF + (pa + NPROD / 2) * 128);
            }
            static_for<14>([&](auto cc) { if (hiss(decltype(cc)::value) == g) cload(decltype(cc)::value); });
            SCHED();
            acc2[1][1] = mfma(fd[cur][1], h1, acc2[1][1]);
            SCHED();
            static_for<NDM>([&](auto dc) { constexpr int d = decltype(dc)::value; if (diss(d) == g) id1(dpart(d), dchunk(d), d & 3); });
            if (g == PB) poll(vb, FIN_OFF + (pa + NPROD / 2) * 128);
            SCHED();
        });
        {
#pragma unroll
            for (int st = 0; st < 2; ++st) {
                const int row = st * 32 + (lane & 31);
                const unsigned pc = lbase + (row >> 4) * SLOT + w * 4096 + (row & 15) * 256 + ((hh ^ (row & 15)) << 4);
#pragma unroll
                for (int nt = 0; nt < 2; ++nt)
#pragma unroll
                    for (int g = 0; g < 4; ++g) {
                        f32x4 v = {acc2[st][nt][4 * g], acc2[st][nt][4 * g + 1], acc2[st][nt][4 * g + 2], acc2[st][nt][4 * g + 3]};
                        *(f32x4 LAS*)(pc ^ (unsigned)((nt * 8 + g * 2) << 4)) = v;
                    }
            }
        }
        asm volatile(".Lglu_e2:");
    }
    BAR_LGKM();
    {
        const int row = tid >> 3, seg = tid & 7;
        const unsigned a0_ = lbase + (row >> 4) * SLOT + (row & 15) * 256 + (((2 * seg) ^ (row & 15)) << 4), a1_ = a0_ ^ 16u;
        f32x4 lo = *(const f32x4 LAS*)(a0_), hi = *(const f32x4 LAS*)(a1_);
        lo += *(const f32x4 LAS*)(a0_ + 4096); hi += *(const f32x4 LAS*)(a1_ + 4096);
        lo += *(const f32x4 LAS*)(a0_ + 8192); hi += *(const f32x4 LAS*)(a1_ + 8192);
        lo += *(const f32x4 LAS*)(a0_ + 12288); hi += *(const f32x4 LAS*)(a1_ + 12288);
        u32x4 o;
        o.x = pack_bf16(lo[0], lo[1]); o.y = pack_bf16(lo[2], lo[3]);
        o.z = pack_bf16(hi[0], hi[1]); o.w = pack_bf16(hi[2], hi[3]);
        const __amdgpu_buffer_rsrc_t ro = __builtin_amdgcn_make_buffer_rsrc(KPTR(kb, 0), 0, S * D * 2, 0x00020000);
        const int so = __builtin_amdgcn_readfirstlane(mb * 64 * (D * 2) + nb * 128);
        __builtin_amdgcn_raw_buffer_store_b128(o, ro, row * (D * 2) + seg * 16, so, 16);
    }
    asm volatile(".Lglu_e3:");

}

}

extern "C" void kernel_launch(void* const* d_in, const int* in_sizes, int n_in, void* d_out, int out_size, void* d_ws,
                              size_t ws_size, hipStream_t stream) {
    (void)in_sizes; (void)n_in; (void)out_size; (void)ws_size;
    glu_fused<<<dim3(NWG + NHELP), 512, 0, stream>>>(d_in[0], d_in[1], d_in[2], d_in[3], d_out, d_ws);
}
```
